```python
import torch

def _softmax(x, axis=-1):
    return torch.softmax(x, dim=axis)
def _gelu(x):
    return torch.nn.functional.gelu(x, approximate="tanh")
def _silu(x):
    return torch.nn.functional.silu(x)
def _sigmoid(x):
    return torch.sigmoid(x)
def _layernorm(x, axis=-1, eps=1e-5):
    m = x.mean(dim=axis, keepdim=True)
    v = x.var(dim=axis, keepdim=True, unbiased=False)
    return (x - m) / torch.sqrt(v + eps)
def _scatter_add(tgt, idx, src, axis=0):
    return tgt.clone().index_add_(axis, idx, src)
def _segment_sum(src, seg, n):
    out = torch.zeros((n,) + src.shape[1:], dtype=src.dtype)
    return out.index_add_(0, seg, src)
def _pad(x, pw):
    flat = tuple(v for pair in reversed(pw) for v in pair)
    return torch.nn.functional.pad(x, flat)

kernel_name = 'interleave_layers__sliding_block__glu_gated_glu__prompt'

def setup_inputs():

    _w = (lambda a: (lambda t: t.to(torch.bfloat16) if t.is_floating_point() else t)(torch.as_tensor(a)))
    def _A_setup():
        B, S, D = 1, 2048, 640
        return {
            "x": _w((lambda *s: torch.randn(*s, dtype=torch.bfloat16) * 0.05)(B, S, D)),
            "wqkv": _w((lambda *s: torch.randn(*s, dtype=torch.bfloat16) * 0.05)(D, 3 * D)),
            "wo": _w((lambda *s: torch.randn(*s, dtype=torch.bfloat16) * 0.05)(D, D)),
        }
    _A_in = _A_setup()

    def _B_setup():
        B, S, D, F = 1, 2048, 640, 1536
        return {
            "x": _w((lambda *s: torch.randn(*s, dtype=torch.bfloat16) * 0.05)(B, S, D)),
            "w_gate": _w((lambda *s: torch.randn(*s, dtype=torch.bfloat16) * 0.05)(D, F)),
            "w_up": _w((lambda *s: torch.randn(*s, dtype=torch.bfloat16) * 0.05)(D, F)),
            "w_down": _w((lambda *s: torch.randn(*s, dtype=torch.bfloat16) * 0.05)(F, D)),
        }
    _B_in = _B_setup()

    return {
        "A_x": _A_in["x"],
        "A_wqkv": _A_in["wqkv"],
        "A_wo": _A_in["wo"],
        "B_x": _B_in["x"],
        "B_w_gate": _B_in["w_gate"],
        "B_w_up": _B_in["w_up"],
        "B_w_down": _B_in["w_down"],
    }

def reference(A_x, A_wqkv, A_wo, B_x, B_w_gate, B_w_up, B_w_down):
    def _A_ref(x, wqkv, wo):
        B, S, D = x.shape
        H = 32; Dh = D // H; W = 256
        qkv = (lambda x, *d: x.permute(*d))((x @ wqkv).reshape(B, S, 3, H, Dh), 2, 0, 3, 1, 4)
        q, k, v = qkv[0], qkv[1], qkv[2]
        scores = torch.einsum("bhqd,bhkd->bhqk", q, k) * (Dh ** -0.5)
        idx = torch.arange(S)
        mask = (idx[:, None] - idx[None, :] >= 0) & (idx[:, None] - idx[None, :] < W)
        scores = torch.where(mask[None, None], scores, -1e9)
        p = _softmax(scores, axis=-1)
        o = torch.einsum("bhqk,bhkd->bhqd", p, v)
        return (lambda x, *d: x.permute(*d))(o, 0, 2, 1, 3).reshape(B, S, D) @ wo

    def _B_ref(x, w_gate, w_up, w_down):
        g = torch.einsum("bsd,df->bsf", x, w_gate)
        u = torch.einsum("bsd,df->bsf", x, w_up)
        h = _sigmoid(g) * u
        return torch.einsum("bsf,fd->bsd", h, w_down)

    x = A_x
    x = x + _A_ref(_layernorm(x, axis=-1), A_wqkv, A_wo)
    x = x + _B_ref(_layernorm(x, axis=-1), B_w_gate, B_w_up, B_w_down)
    x = x + _A_ref(_layernorm(x, axis=-1), A_wqkv, A_wo)
    x = x + _B_ref(_layernorm(x, axis=-1), B_w_gate, B_w_up, B_w_down)
    return x
```

```cpp
#include <hip/hip_runtime.h>
#include <hip/hip_bf16.h>
#include <stdint.h>
typedef unsigned short u16;
__device__ __forceinline__ float bf2f(u16 b){ return __builtin_bit_cast(float,(unsigned)b<<16); }
__device__ __forceinline__ u16 f2bf(float f){ __hip_bfloat16 h = __float2bfloat16(f); return __builtin_bit_cast(u16,h); }
__device__ __forceinline__ float rbf(float f){ return bf2f(f2bf(f)); }
constexpr int S=2048, D=640, H=32, DH=20, W=256, F=1536;

__global__ void k_ln(const u16* x, u16* y){
  int row=blockIdx.x; int t=threadIdx.x;
  float v[10]; float s=0;
  for(int i=0;i<10;i++){ v[i]=bf2f(x[row*D+t+64*i]); s+=v[i]; }
  for(int o=32;o>=1;o>>=1) s+=__shfl_xor(s,o);
  float mean=s/D; float q=0;
  for(int i=0;i<10;i++){ float d=v[i]-mean; q+=d*d; }
  for(int o=32;o>=1;o>>=1) q+=__shfl_xor(q,o);
  float var=q/D;
  float mb=rbf(mean), vb=rbf(var);
  float den=rbf(sqrtf(rbf(vb+1e-5f)));
  for(int i=0;i<10;i++){ y[row*D+t+64*i]=f2bf(rbf(v[i]-mb)/den); }
}
__global__ void k_gemm(const u16* A,const u16* B,u16* C,const u16* res,int M,int N,int K,int mode){
  __shared__ float As[32][33], Bs[32][33];
  int tx=threadIdx.x, ty=threadIdx.y; int row=blockIdx.y*32+ty, col=blockIdx.x*32+tx;
  float acc=0;
  for(int k0=0;k0<K;k0+=32){
    As[ty][tx]=bf2f(A[row*K+k0+tx]); Bs[ty][tx]=bf2f(B[(k0+ty)*N+col]);
    __syncthreads();
    for(int k=0;k<32;k++) acc+=As[ty][k]*Bs[k][tx];
    __syncthreads();
  }
  if(mode==0) C[row*N+col]=f2bf(acc);
  else C[row*N+col]=f2bf(bf2f(res[row*N+col])+rbf(acc));
}
__global__ void k_attn(const u16* qkv,u16* o){
  int q=blockIdx.x, h=blockIdx.y, t=threadIdx.x;
  __shared__ float p[256]; __shared__ float red[4];
  int key=q-t; float s=-1e30f;
  if(key>=0){ float a=0; for(int d=0;d<DH;d++) a+=bf2f(qkv[q*3*D+h*DH+d])*bf2f(qkv[key*3*D+D+h*DH+d]); s=rbf(rbf(a)*0.22360679774997896f); }
  float m=s; for(int o2=32;o2>=1;o2>>=1) m=fmaxf(m,__shfl_xor(m,o2));
  if((t&63)==0) red[t>>6]=m; __syncthreads();
  m=fmaxf(fmaxf(red[0],red[1]),fmaxf(red[2],red[3])); __syncthreads();
  float e= key>=0? __expf(s-m):0.f; float l=e;
  for(int o2=32;o2>=1;o2>>=1) l+=__shfl_xor(l,o2);
  if((t&63)==0) red[t>>6]=l; __syncthreads();
  l=red[0]+red[1]+red[2]+red[3];
  p[t]=rbf(e/l); __syncthreads();
  if(t<DH){ float a=0; for(int j=0;j<256;j++){ int kk=q-j; if(kk>=0) a+=p[j]*bf2f(qkv[kk*3*D+2*D+h*DH+t]); } o[q*D+h*DH+t]=f2bf(a); }
}
__global__ void k_glu(const u16* g,const u16* u,u16* h,int n){
  int i=blockIdx.x*256+threadIdx.x; if(i<n){ float gv=bf2f(g[i]); float sg=rbf(1.f/(1.f+__expf(-gv))); h[i]=f2bf(sg*bf2f(u[i])); }
}
extern "C" void kernel_launch(void* const* d_in, const int* in_sizes, int n_in,
                              void* d_out, int out_size, void* d_ws, size_t ws_size,
                              hipStream_t stream) {
  const u16* x=(const u16*)d_in[0]; const u16* wqkv=(const u16*)d_in[1]; const u16* wo=(const u16*)d_in[2];
  const u16* wg=(const u16*)d_in[4]; const u16* wu=(const u16*)d_in[5]; const u16* wd=(const u16*)d_in[6];
  u16* ws=(u16*)d_ws; u16* xn=ws; u16* qkv=xn+S*D; u16* o=qkv+S*3*D; u16* xa=o+S*D; u16* xb=xa+S*D; u16* g=xb+S*D; u16* u=g+S*F; u16* h=u+S*F;
  const u16* cur=x;
  for(int l=0;l<2;l++){
    k_ln<<<S,64,0,stream>>>(cur,xn);
    k_gemm<<<dim3(3*D/32,S/32),dim3(32,32),0,stream>>>(xn,wqkv,qkv,nullptr,S,3*D,D,0);
    k_attn<<<dim3(S,H),256,0,stream>>>(qkv,o);
    k_gemm<<<dim3(D/32,S/32),dim3(32,32),0,stream>>>(o,wo,xa,cur,S,D,D,1);
    k_ln<<<S,64,0,stream>>>(xa,xn);
    k_gemm<<<dim3(F/32,S/32),dim3(32,32),0,stream>>>(xn,wg,g,nullptr,S,F,D,0);
    k_gemm<<<dim3(F/32,S/32),dim3(32,32),0,stream>>>(xn,wu,u,nullptr,S,F,D,0);
    k_glu<<<S*F/256,256,0,stream>>>(g,u,h,S*F);
    u16* dst = l==1? (u16*)d_out : xb;
    k_gemm<<<dim3(D/32,S/32),dim3(32,32),0,stream>>>(h,wd,dst,xa,S,D,F,1);
    cur=xb;
  }
}
```

```cpp
#include <hip/hip_runtime.h>
#include <hip/hip_bf16.h>
#include <stdint.h>

typedef unsigned short u16;
typedef unsigned v4u __attribute__((ext_vector_type(4)));
typedef unsigned v2u __attribute__((ext_vector_type(2)));
typedef float f32x4 __attribute__((ext_vector_type(4)));
typedef float f32x2 __attribute__((ext_vector_type(2)));
typedef short s16x8 __attribute__((ext_vector_type(8)));
typedef short s16x4 __attribute__((ext_vector_type(4)));
typedef __bf16 bf16x2 __attribute__((ext_vector_type(2)));
typedef __bf16 bf16x8 __attribute__((ext_vector_type(8)));
#define GAS __attribute__((address_space(1)))
#define LAS __attribute__((address_space(3)))
#define DI __device__ __forceinline__
#define RLX_AGENT __ATOMIC_RELAXED, __HIP_MEMORY_SCOPE_AGENT
typedef GAS unsigned long long gu64;

constexpr int S_ = 2048, D_ = 640, F_ = 1536;
constexpr unsigned FLAG_OFF = 0, FLAG_BYTES = 65536;
constexpr unsigned WQ_OFF = FLAG_BYTES;
constexpr unsigned WO_OFF = WQ_OFF + 16 * 160 * 1024;
constexpr unsigned WG_OFF = WO_OFF + 8 * 100 * 1024;
constexpr unsigned WD_OFF = WG_OFF + 16 * 240 * 1024;
constexpr unsigned CS_OFF = WD_OFF + 8 * 240 * 1024;
constexpr unsigned ACT_OFF = CS_OFF + 32768;
constexpr unsigned KV_BYTES = 16 * 16 * 24 * 1024;
constexpr unsigned XF_BYTES = 16 * 160 * 1024;
constexpr unsigned HF_BYTES = 16 * 384 * 1024;
constexpr unsigned L_KV = 0, L_O = L_KV + KV_BYTES, L_X2 = L_O + XF_BYTES, L_H = L_X2 + XF_BYTES, L_X3 = L_H + HF_BYTES,
                   LAYER_BYTES = L_X3 + XF_BYTES;
constexpr unsigned WS_USED = ACT_OFF + 2 * LAYER_BYTES;
constexpr unsigned STAMP_OFF = WS_USED;
enum { PH_KV = 0, PH_O = 1, PH_X2 = 2, PH_H = 3, PH_X3 = 4 };

constexpr int LDS_RING = 0;
constexpr int LDS_KV = 0;
constexpr int TP = 3072;
constexpr int LDS_Q = 24 * TP;
constexpr int LDS_RED = 0;
constexpr int RP = 336;
constexpr int LDS_H = 131072;
constexpr int HP = 208;
constexpr int LDS_ST = 157696;
constexpr int LDS_BYTES = LDS_ST + 1024;

DI float bf2f(unsigned b16) { return __builtin_bit_cast(float, b16 << 16); }
DI unsigned pk2(float a, float b) {
  bf16x2 r; r[0] = (__bf16)a; r[1] = (__bf16)b; return __builtin_bit_cast(unsigned, r);
}
DI float rbf(float a) { return bf2f(pk2(a, 0.f) & 0xffffu); }
DI f32x4 mfma(v4u a, v4u b, f32x4 c) {
  return __builtin_amdgcn_mfma_f32_16x16x32_bf16(__builtin_bit_cast(bf16x8, a), __builtin_bit_cast(bf16x8, b), c, 0, 0, 0);
}
DI float dot2(unsigned a, unsigned b, float c) {
  return __builtin_amdgcn_fdot2_f32_bf16(__builtin_bit_cast(bf16x2, a), __builtin_bit_cast(bf16x2, b), c, false);
}
DI v4u ldb(__amdgpu_buffer_rsrc_t rs, unsigned voff, unsigned soff) { return __builtin_amdgcn_raw_buffer_load_b128(rs, voff, soff, 0); }
DI v4u ldc(__amdgpu_buffer_rsrc_t rs, unsigned voff, unsigned soff) { return __builtin_amdgcn_raw_buffer_load_b128(rs, voff, soff, 16); }
DI void stc(v4u v, __amdgpu_buffer_rsrc_t rs, unsigned voff, unsigned soff) { __builtin_amdgcn_raw_buffer_store_b128(v, rs, voff, soff, 16); }
DI void dma(__amdgpu_buffer_rsrc_t rs, unsigned voff, unsigned soff, unsigned lds) {
  unsigned keep;
  asm volatile("s_mov_b32 %0, m0\n\ts_mov_b32 m0, %4\n\ts_nop 0\n\tbuffer_load_dwordx4 %1, %2, %3 offen sc1 lds\n\ts_mov_b32 m0, %0"
               : "=&s"(keep) : "v"(voff), "s"(rs), "s"(soff), "s"(lds) : "memory");
}
#define OPAQUE(v) asm volatile("" : "+s"(v))
#define FRESH() int tid = tid0; asm volatile("" : "+v"(tid)); const int lane = tid & 63, r = tid & 15, gq = (tid >> 4) & 3; const unsigned l16 = lane * 16; (void)r; (void)gq; (void)l16
#define VMCNT(n) asm volatile("s_waitcnt vmcnt(" #n ")" ::: "memory")
#define BAR() asm volatile("s_waitcnt lgkmcnt(0)\n\ts_barrier" ::: "memory")
#define SBAR() __builtin_amdgcn_sched_barrier(0)
DI float sum16(float v) { auto t = __builtin_amdgcn_permlane16_swap(__builtin_bit_cast(unsigned, v), __builtin_bit_cast(unsigned, v), false, false);
  return __builtin_bit_cast(float, (unsigned)t[0]) + __builtin_bit_cast(float, (unsigned)t[1]); }
DI float sum32(float v) { auto t = __builtin_amdgcn_permlane32_swap(__builtin_bit_cast(unsigned, v), __builtin_bit_cast(unsigned, v), false, false);
  return __builtin_bit_cast(float, (unsigned)t[0]) + __builtin_bit_cast(float, (unsigned)t[1]); }
DI float max16(float v) { auto t = __builtin_amdgcn_permlane16_swap(__builtin_bit_cast(unsigned, v), __builtin_bit_cast(unsigned, v), false, false);
  return fmaxf(__builtin_bit_cast(float, (unsigned)t[0]), __builtin_bit_cast(float, (unsigned)t[1])); }
DI float max32(float v) { auto t = __builtin_amdgcn_permlane32_swap(__builtin_bit_cast(unsigned, v), __builtin_bit_cast(unsigned, v), false, false);
  return fmaxf(__builtin_bit_cast(float, (unsigned)t[0]), __builtin_bit_cast(float, (unsigned)t[1])); }
DI float bcast_row1(float v) {
  auto t = __builtin_amdgcn_permlane16_swap(__builtin_bit_cast(unsigned, v), __builtin_bit_cast(unsigned, v), false, false);
  auto u = __builtin_amdgcn_permlane32_swap((unsigned)t[1], (unsigned)t[1], false, false);
  return __builtin_bit_cast(float, (unsigned)u[0]);
}

#define STAMP(i)

__global__ void __launch_bounds__(256) k_prep(const u16* __restrict__ wqkv, const u16* __restrict__ wo, const u16* __restrict__ wg,
                                             const u16* __restrict__ wu, const u16* __restrict__ wd, char* __restrict__ ws) {
  __shared__ float red[4][16];
  const int tid = threadIdx.x, lane = tid & 63, w = tid >> 6;
  const int s = blockIdx.x;
  if (s < 16) ((v4u*)(ws + FLAG_OFF))[s * 256 + tid] = v4u{0, 0, 0, 0};
  const int r = lane & 15, gq = lane >> 4;
  const u16* src; int ld, col, nks; bool zero = false; unsigned dst, dstep; int cso = -1;
  if (s < 128) {
    int g = s >> 3, nt = s & 7, pc = 16 * nt + r;
    src = wqkv; ld = 1920; nks = 20;
    if (pc < 40) col = 40 * g + pc;
    else if (pc < 60) col = 640 + 40 * g + (pc - 40);
    else if (pc < 80) col = 1280 + 40 * g + (pc - 60);
    else if (pc < 100) col = 640 + 40 * g + 20 + (pc - 80);
    else if (pc < 120) col = 1280 + 40 * g + 20 + (pc - 100);
    else { col = 0; zero = true; }
    dst = WQ_OFF + (g * 160 + (nt & 1) * 4 + (nt >> 1)) * 1024 + w * 8192; dstep = 4 * 8192; cso = g * 128 + nt * 16;
  } else if (s < 320) {
    int e = s - 128, g = e / 12, wv = (e % 12) / 3, j = e % 3;
    src = (r & 2) ? wu : wg; ld = 1536; nks = 20; col = 96 * g + 24 * wv + 8 * j + 2 * (r >> 2) + (r & 1);
    dst = WG_OFF + (g * 240 + j * 4 + wv) * 1024 + w * 12288; dstep = 4 * 12288; cso = 2048 + g * 192 + (wv * 3 + j) * 16;
  } else if (s < 360) {
    int e = s - 320, c = e / 5, nt = e % 5;
    src = wo; ld = 640; nks = 20; col = 80 * c + 16 * nt + r;
    dst = WO_OFF + (c * 100 + nt * 4 + w) * 1024; dstep = 20 * 1024;
  } else {
    int e = s - 360, c = e / 5, nt = e % 5;
    src = wd; ld = 640; nks = 48; col = 80 * c + 16 * nt + r;
    dst = WD_OFF + (c * 240 + nt * 4 + w) * 1024; dstep = 20 * 1024;
  }
  float cs = 0.f;
  for (int i = 0; i < nks / 4; ++i) {
    const u16* p = src + (size_t)(32 * (4 * i + w) + 8 * gq) * ld + col;
    unsigned e[8];
#pragma unroll
    for (int jj = 0; jj < 8; ++jj) e[jj] = p[jj * ld];
    v4u o = {e[0] | (e[1] << 16), e[2] | (e[3] << 16), e[4] | (e[5] << 16), e[6] | (e[7] << 16)};
    if (zero) o = v4u{0, 0, 0, 0};
#pragma unroll
    for (int q = 0; q < 4; ++q) cs = dot2(o[q], 0x3f803f80u, cs);
    *(v4u*)(ws + dst + i * dstep + lane * 16) = o;
  }
  if (cso >= 0) {
    cs = sum32(sum16(cs));
    if (lane < 16) red[w][lane] = cs;
    __syncthreads();
    if (tid < 16) ((float*)(ws + CS_OFF))[cso + tid] = red[0][tid] + red[1][tid] + red[2][tid] + red[3][tid];
  }
}

DI void wait_flags(gu64* fl, int n, int stride, int wave, int lane) {
  if (wave == 0) {
    for (int spins = 0; spins < (1 << 22); ++spins) {
      unsigned long long v = lane < n ? __hip_atomic_load(fl + lane * stride, RLX_AGENT) : 1ull;
      if (__all(v == 1ull)) break;
    }
  }
  BAR();
}
DI void publish(gu64* fl, int tid) {
  VMCNT(0);
  BAR();
  if (tid == 0) __hip_atomic_store(fl, 1ull, RLX_AGENT);
}

__global__ void __launch_bounds__(256, 1) k_main(const u16* __restrict__ x, u16* __restrict__ out, char* __restrict__ ws) {
  extern __shared__ __attribute__((aligned(16))) char lds[];
  const int tid0 = threadIdx.x;
  const int w = __builtin_amdgcn_readfirstlane(tid0 >> 6);
  const int b = blockIdx.x, x8 = b & 7, rr = b >> 3;
  const int p = (x8 >> 1) * 4 + (rr >> 3), g = (x8 & 1) * 8 + (rr & 7);
  const int rh = g & 1, c = g >> 1;
  const __amdgpu_buffer_rsrc_t rsW = __builtin_amdgcn_make_buffer_rsrc((void*)ws, 0, 0x7fffffff, 0x00020000);
  const __amdgpu_buffer_rsrc_t rsX = __builtin_amdgcn_make_buffer_rsrc((void*)x, 0, S_ * D_ * 2, 0x00020000);
  gu64* flags = (gu64*)(ws + FLAG_OFF);
  const unsigned ONES = 0x3f803f80u;

  v4u Bw[60];
  v4u res[3];
  {
    FRESH();
    unsigned wb = WQ_OFF + (g * 160 + w) * 1024; OPAQUE(wb);
#pragma unroll
    for (int i = 0; i < 40; ++i) Bw[i] = ldb(rsW, l16, wb + i * 4096);
#pragma unroll
    for (int i = 0; i < 3; ++i) {
      int jc = w + 4 * i; if (jc > 9) jc = 9;
      res[i] = ldb(rsX, (unsigned)lane * 1280u, (unsigned)((128 * p + 64 * rh) * 1280 + (80 * c + 8 * jc) * 2));
    }
  }

#pragma unroll 1
  for (int layer = 0; layer < 2; ++layer) {
    const unsigned LB = ACT_OFF + layer * LAYER_BYTES;
    gu64* fl = flags + layer * 5 * 256;
    {
      FRESH();
      STAMP(0);
      f32x4 c4[2];
#pragma unroll
      for (int j = 0; j < 2; ++j) c4[j] = __builtin_bit_cast(f32x4, ldb(rsW, gq * 16, CS_OFF + (g * 128 + (2 * w + j) * 16) * 4));
      __amdgpu_buffer_rsrc_t rsA = layer == 0 ? rsX : rsW;
      unsigned avoff, abase, sa, sb;
      if (layer == 0) { avoff = r * 1280 + gq * 16; abase = 128 * p * 1280; sa = 20480; sb = 64; }
      else {
        wait_flags(flags + PH_X3 * 256 + p * 16, 16, 1, w, lane);
        avoff = l16; abase = ACT_OFF + L_X3 + p * 163840; sa = 1024; sb = 8192;
      }
      STAMP(1);
      abase += 2 * w * sa; unsigned lb = LDS_RING + 2 * w * 1024;
      OPAQUE(abase); OPAQUE(lb); OPAQUE(sa); OPAQUE(sb);
#pragma unroll
      for (int ks = 0; ks < 16; ++ks)
#pragma unroll
        for (int m = 0; m < 2; ++m) dma(rsA, avoff, abase + m * sa + ks * sb, lb + ks * 8192 + m * 1024);
      f32x4 acc[2][8];
#pragma unroll
      for (int j = 0; j < 2; ++j)
#pragma unroll
        for (int m = 0; m < 8; ++m) acc[j][m] = f32x4{0, 0, 0, 0};
      float ssum[2] = {0, 0}, ssq[2] = {0, 0};
      unsigned am[8];
#pragma unroll
      for (int m = 0; m < 8; ++m) am[m] = (((m + 2 * w) & 7) * 1024) + l16;
      v4u xa[2][8];
      VMCNT(24);
      BAR();
#pragma unroll
      for (int m = 0; m < 8; ++m) xa[0][m] = *(const v4u*)(lds + LDS_RING + am[m]);
#pragma unroll
      for (int ks = 0; ks < 20; ++ks) {
        if (ks == 3) {
          VMCNT(16); BAR();
#pragma unroll
          for (int k2 = 16; k2 < 20; ++k2)
#pragma unroll
            for (int m = 0; m < 2; ++m) dma(rsA, avoff, abase + m * sa + k2 * sb, lb + (k2 - 16) * 8192 + m * 1024);
        }
        if (ks == 7) { VMCNT(16); BAR(); }
        if (ks == 11) { VMCNT(8); BAR(); }
        if (ks == 15) { VMCNT(0); BAR(); }
#pragma unroll
        for (int m = 0; m < 8; ++m) {
          if (ks < 19) xa[(ks + 1) & 1][m] = *(const v4u*)(lds + LDS_RING + ((ks + 1) & 15) * 8192 + am[m]);
#pragma unroll
          for (int j = 0; j < 2; ++j) acc[j][m] = mfma(Bw[ks * 2 + j], xa[ks & 1][m], acc[j][m]);
          if (m >= 2 && m < 6) {
            const int mm = (m - 2) >> 1, e0 = ((m - 2) & 1) * 2;
#pragma unroll
            for (int e = e0; e < e0 + 2; ++e) { ssum[mm] = dot2(xa[ks & 1][mm][e], ONES, ssum[mm]); ssq[mm] = dot2(xa[ks & 1][mm][e], xa[ks & 1][mm][e], ssq[mm]); }
          }
          __builtin_amdgcn_sched_barrier(0);
        }
      }
      STAMP(2);
#pragma unroll
      for (int m = 0; m < 2; ++m) {
        float s = sum32(sum16(ssum[m]));
        float q = sum32(sum16(ssq[m]));
        float mean = s * (1.f / 640.f);
        float var = fmaxf(q * (1.f / 640.f) - mean * mean, 0.f);
        float rstd = rsqrtf(var + 1e-5f);
        if (lane < 16) *(f32x2*)(lds + LDS_ST + ((2 * w + m) * 16 + r) * 8) = f32x2{mean, rstd};
      }
      BAR();
      if (tid < 8) *(v4u*)(lds + LDS_KV + (16 + tid) * TP + 2560) = v4u{0x00003f80u, 0, 0, 0};
      if (p < 2) {
        const int nz = (2 - p) * 8 * TP / 16;
        for (int i = tid; i < nz; i += 256) *(v4u*)(lds + LDS_KV + i * 16) = v4u{0, 0, 0, 0};
      }
      {
        f32x2 st[8];
#pragma unroll
        for (int m = 0; m < 8; ++m) st[m] = *(const f32x2*)(lds + LDS_ST + ((((m + 2 * w) & 7) * 16) + r) * 8);
        SBAR();
#pragma unroll
        for (int j = 0; j < 2; ++j) {
          const int pc0 = 16 * (2 * w + j) + 4 * gq;
          const int a0 = pc0 < 40 ? LDS_Q + r * 80 + pc0 * 2 : (pc0 < 120 ? LDS_KV + 16 * TP + r * 160 + (pc0 - 40) * 2 : LDS_KV + 16 * TP + 2688 + r * 8);
          const int ts = pc0 < 40 ? 1280 : TP;
#pragma unroll
          for (int m = 0; m < 8; ++m) {
            const int mt = (m + 2 * w) & 7;
            float y0 = st[m][1] * (acc[j][m][0] - st[m][0] * c4[j][0]), y1 = st[m][1] * (acc[j][m][1] - st[m][0] * c4[j][1]);
            float y2 = st[m][1] * (acc[j][m][2] - st[m][0] * c4[j][2]), y3 = st[m][1] * (acc[j][m][3] - st[m][0] * c4[j][3]);
            *(v2u*)(lds + a0 + mt * ts) = v2u{pk2(y0, y1), pk2(y2, y3)};
          }
        }
      }
      BAR();
      {
        v4u v[6];
#pragma unroll
        for (int i = 0; i < 6; ++i) v[i] = *(const v4u*)(lds + LDS_KV + 16 * TP + (tid + 256 * i) * 16);
        SBAR();
#pragma unroll
        for (int i = 0; i < 6; ++i) stc(v[i], rsW, (unsigned)(tid + 256 * i) * 16, LB + L_KV + (p * 16 + g) * 24576);
      }
      publish(fl + PH_KV * 256 + p * 16 + g, tid);
      STAMP(3);
    }
    {
      FRESH();
      if (p >= 1) {
        wait_flags(fl + PH_KV * 256 + (p >= 2 ? (p - 2) : (p - 1)) * 16 + g, p >= 2 ? 2 : 1, 16, w, lane);
        unsigned kb = LB + L_KV + ((p - 1) * 16 + g) * 24576 + w * 1024, lb = LDS_KV + w * 1024;
        OPAQUE(kb); OPAQUE(lb);
#pragma unroll
        for (int i = 0; i < 6; ++i) dma(rsW, l16, kb + i * 4096, lb + 8 * TP + i * 4096);
        if (p >= 2) {
#pragma unroll
          for (int i = 0; i < 6; ++i) dma(rsW, l16, kb - 16 * 24576 + i * 4096, lb + i * 4096);
        }
      }
      VMCNT(0);
      BAR();
      STAMP(4);
      {
        unsigned wb = WO_OFF + (c * 100 + w) * 1024; OPAQUE(wb);
#pragma unroll
        for (int i = 0; i < 25; ++i) Bw[35 + i] = ldb(rsW, l16, wb + i * 4096);
        unsigned wb2 = WG_OFF + (g * 240 + w) * 1024; OPAQUE(wb2);
#pragma unroll
        for (int i = 0; i < 35; ++i) Bw[i] = ldb(rsW, l16, wb2 + i * 4096);
      }
      asm volatile("" ::: "memory");
      const int hh = w & 1;
      const int tmin = p >= 2 ? 0 : (p == 1 ? 8 : 16);
      const float SC = 0.22360679774997896f * 1.4426950408889634f;
      const int q4 = (lane & 15) >> 2, p4 = lane & 3;
      const unsigned koff = r * 160 + hh * 80 + 16 * (gq < 2 ? gq : 2);
      const unsigned voff0 = (4 * gq + q4) * 160 + hh * 80 + 40 + 8 * p4;
      const unsigned voff1 = p4 == 0 ? (4 * gq + q4) * 160 + hh * 80 + 40 + 32 : (p4 == 1 ? 2560 : 2568);
      bool up[4];
#pragma unroll
      for (int e = 0; e < 4; ++e) up[e] = (4 * gq + e) > r;
      const unsigned mA0 = (up[0] ? 0xffffu : 0u) | (up[1] ? 0xffff0000u : 0u), mA1 = (up[2] ? 0xffffu : 0u) | (up[3] ? 0xffff0000u : 0u);
#pragma unroll 1
      for (int u = 0; u < 4; ++u) {
        const int j = (w >> 1) * 4 + u;
        char* qrow = lds + LDS_Q + (16 * j + r) * 80 + hh * 40;
        v2u qlo = *(const v2u*)(qrow + (gq < 2 ? 16 * gq : 32));
        v2u qhi = *(const v2u*)(qrow + (gq < 2 ? 16 * gq + 8 : 32));
        v4u qf = {gq < 3 ? qlo[0] : 0u, gq < 3 ? qlo[1] : 0u, gq < 2 ? qhi[0] : 0u, gq < 2 ? qhi[1] : 0u};
        const char* kvb = lds + LDS_KV + j * TP;
        f32x4 s[17];
        {
          v4u kf[17];
#pragma unroll
          for (int t = 0; t < 17; ++t) kf[t] = *(const v4u*)(kvb + t * TP + koff);
          __builtin_amdgcn_sched_barrier(0);
#pragma unroll
          for (int t = 0; t < 17; ++t) {
            float c0 = (j + t >= tmin) ? 0.f : -1e30f;
            s[t] = mfma(kf[t], qf, f32x4{c0, c0, c0, c0});
          }
          __builtin_amdgcn_sched_barrier(0);
        }
        v2u v0[17], v1[17];
#pragma unroll
        for (int t = 0; t < 17; ++t) {
          v0[t] = __builtin_bit_cast(v2u, __builtin_amdgcn_ds_read_tr16_b64_v4i16((LAS s16x4*)(kvb + t * TP + voff0)));
          v1[t] = __builtin_bit_cast(v2u, __builtin_amdgcn_ds_read_tr16_b64_v4i16((LAS s16x4*)(kvb + t * TP + voff1)));
        }
        __builtin_amdgcn_sched_barrier(0);
#pragma unroll
        for (int e = 0; e < 4; ++e) s[0][e] = up[e] ? s[0][e] : s[16][e];
        float m = -3e38f;
#pragma unroll
        for (int t = 0; t < 16; ++t) m = fmaxf(fmaxf(m, fmaxf(s[t][0], s[t][1])), fmaxf(s[t][2], s[t][3]));
        m = max32(max16(m));
        const float mc = -m * SC;
        unsigned pp[16][2];
#pragma unroll
        for (int t = 0; t < 16; ++t) {
          float e0 = __builtin_amdgcn_exp2f(fmaf(s[t][0], SC, mc)), e1 = __builtin_amdgcn_exp2f(fmaf(s[t][1], SC, mc));
          float e2 = __builtin_amdgcn_exp2f(fmaf(s[t][2], SC, mc)), e3 = __builtin_amdgcn_exp2f(fmaf(s[t][3], SC, mc));
          pp[t][0] = pk2(e0, e1); pp[t][1] = pk2(e2, e3);
        }
        f32x4 o0 = {0, 0, 0, 0}, o1 = {0, 0, 0, 0};
#pragma unroll
        for (int pr = 0; pr < 9; ++pr) {
          const int ta = pr == 0 ? 0 : 2 * pr - 1, tb = pr == 0 ? 16 : (pr == 8 ? 15 : 2 * pr);
          v4u pf;
          if (pr == 0) pf = v4u{pp[0][0] & mA0, pp[0][1] & mA1, pp[0][0] & ~mA0, pp[0][1] & ~mA1};
          else if (pr == 8) pf = v4u{pp[15][0], pp[15][1], 0u, 0u};
          else pf = v4u{pp[ta][0], pp[ta][1], pp[tb][0], pp[tb][1]};
          o0 = mfma(v4u{v0[ta][0], v0[ta][1], v0[tb][0], v0[tb][1]}, pf, o0);
          o1 = mfma(v4u{v1[ta][0], v1[ta][1], v1[tb][0], v1[tb][1]}, pf, o1);
        }
        const float l = bcast_row1(o1[0]);
        const float inv = 1.f / l;
        *(v2u*)(qrow + 8 * gq) = v2u{pk2(o0[0] * inv, o0[1] * inv), pk2(o0[2] * inv, o0[3] * inv)};
        if (gq == 0) *(v2u*)(qrow + 32) = v2u{pk2(o1[0] * inv, o1[1] * inv), pk2(o1[2] * inv, o1[3] * inv)};
      }
      BAR();
      STAMP(5);
      {
        v4u v[3];
#pragma unroll
        for (int i = 0; i < 3; ++i) {
          const int id = tid + 256 * i, mt = (id >> 4) & 7, jc = i < 2 ? (id >> 7) : 4;
          v[i] = *(const v4u*)(lds + LDS_Q + (16 * mt + r) * 80 + 16 * jc);
        }
        SBAR();
#pragma unroll
        for (int i = 0; i < 3; ++i) {
          const int id = tid + 256 * i, mt = (id >> 4) & 7, jc = i < 2 ? (id >> 7) : 4;
          const int c8 = 5 * g + jc;
          if (i < 2 || w < 2)
            stc(v[i], rsW, (unsigned)((((mt >> 2) * 20 + (c8 >> 2)) * 4 + (mt & 3)) * 1024 + (c8 & 3) * 256 + r * 16), LB + L_O + p * 163840);
        }
      }
      publish(fl + PH_O * 256 + p * 16 + g, tid);
      STAMP(6);
    }
    {
      FRESH();
      wait_flags(fl + PH_O * 256 + p * 16, 16, 1, w, lane);
      STAMP(7);
      v4u af[20];
      unsigned ob = LB + L_O + p * 163840 + (rh * 20 + w) * 4096; OPAQUE(ob);
#pragma unroll
      for (int i = 0; i < 5; ++i)
#pragma unroll
        for (int m = 0; m < 4; ++m) af[i * 4 + m] = ldc(rsW, l16 + m * 1024, ob + i * 16384);
      SBAR();
      f32x4 acc[5][4];
#pragma unroll
      for (int n = 0; n < 5; ++n)
#pragma unroll
        for (int m = 0; m < 4; ++m) acc[n][m] = f32x4{0, 0, 0, 0};
#pragma unroll
      for (int i = 0; i < 5; ++i)
#pragma unroll
        for (int m = 0; m < 4; ++m)
#pragma unroll
          for (int n = 0; n < 5; ++n) acc[n][m] = mfma(Bw[35 + i * 5 + n], af[i * 4 + m], acc[n][m]);
#pragma unroll
      for (int n = 0; n < 5; ++n)
#pragma unroll
        for (int m = 0; m < 4; ++m) *(f32x4*)(lds + LDS_RED + (w * 64 + 16 * m + r) * RP + (16 * n + 4 * gq) * 4) = acc[n][m];
      BAR();
      STAMP(8);
      f32x4 rd[3][4][2];
#pragma unroll
      for (int i = 0; i < 3; ++i) {
        const int jc = (i < 2 || w < 2) ? w + 4 * i : w;
#pragma unroll
        for (int q = 0; q < 4; ++q) {
          rd[i][q][0] = *(const f32x4*)(lds + LDS_RED + (q * 64 + lane) * RP + jc * 32);
          rd[i][q][1] = *(const f32x4*)(lds + LDS_RED + (q * 64 + lane) * RP + jc * 32 + 16);
        }
      }
      SBAR();
#pragma unroll
      for (int i = 0; i < 3; ++i) {
        const int jc = w + 4 * i;
        if (jc < 10) {
          f32x4 s0 = (rd[i][0][0] + rd[i][1][0]) + (rd[i][2][0] + rd[i][3][0]), s1 = (rd[i][0][1] + rd[i][1][1]) + (rd[i][2][1] + rd[i][3][1]);
          v4u o;
#pragma unroll
          for (int e = 0; e < 2; ++e) {
            o[e] = pk2(bf2f(res[i][e] & 0xffffu) + rbf(s0[2 * e]), bf2f(res[i][e] >> 16) + rbf(s0[2 * e + 1]));
            o[2 + e] = pk2(bf2f(res[i][2 + e] & 0xffffu) + rbf(s1[2 * e]), bf2f(res[i][2 + e] >> 16) + rbf(s1[2 * e + 1]));
          }
          res[i] = o;
          const int c8 = 10 * c + jc;
          stc(o, rsW, (unsigned)((lane >> 4) * 1024 + r * 16), LB + L_X2 + p * 163840 + ((c8 >> 2) * 8 + 4 * rh) * 1024 + (c8 & 3) * 256);
        }
      }
      publish(fl + PH_X2 * 256 + p * 16 + g, tid);
      STAMP(9);
    }
    {
      FRESH();
      f32x4 c4[3];
#pragma unroll
      for (int j = 0; j < 3; ++j) c4[j] = __builtin_bit_cast(f32x4, ldb(rsW, gq * 16, CS_OFF + (2048 + g * 192 + (3 * w + j) * 16) * 4));
      wait_flags(fl + PH_X2 * 256 + p * 16, 16, 1, w, lane);
      STAMP(10);
      unsigned abase = LB + L_X2 + p * 163840 + 2 * w * 1024, lb = LDS_RING + 2 * w * 1024;
      OPAQUE(abase); OPAQUE(lb);
#pragma unroll
      for (int ks = 0; ks < 16; ++ks)
#pragma unroll
        for (int m = 0; m < 2; ++m) dma(rsW, l16, abase + m * 1024 + ks * 8192, lb + ks * 8192 + m * 1024);
      {
        unsigned wb2 = WG_OFF + (g * 240 + w) * 1024; OPAQUE(wb2);
#pragma unroll
        for (int i = 35; i < 60; ++i) Bw[i] = ldb(rsW, l16, wb2 + i * 4096);
      }
      asm volatile("" ::: "memory");
      f32x4 acc[3][8];
#pragma unroll
      for (int j = 0; j < 3; ++j)
#pragma unroll
        for (int m = 0; m < 8; ++m) acc[j][m] = f32x4{0, 0, 0, 0};
      float ssum[2] = {0, 0}, ssq[2] = {0, 0};
      unsigned am[8];
#pragma unroll
      for (int m = 0; m < 8; ++m) am[m] = (((m + 2 * w) & 7) * 1024) + l16;
      v4u xa[2][8];
      VMCNT(49);
      BAR();
#pragma unroll
      for (int m = 0; m < 8; ++m) xa[0][m] = *(const v4u*)(lds + LDS_RING + am[m]);
#pragma unroll
      for (int ks = 0; ks < 20; ++ks) {
        if (ks == 3) {
          VMCNT(41); BAR();
#pragma unroll
          for (int k2 = 16; k2 < 20; ++k2)
#pragma unroll
            for (int m = 0; m < 2; ++m) dma(rsW, l16, abase + m * 1024 + k2 * 8192, lb + (k2 - 16) * 8192 + m * 1024);
        }
        if (ks == 7) { VMCNT(41); BAR(); }
        if (ks == 11) { VMCNT(33); BAR(); }
        if (ks == 15) { VMCNT(0); BAR(); }
#pragma unroll
        for (int m = 0; m < 8; ++m) {
          if (ks < 19) xa[(ks + 1) & 1][m] = *(const v4u*)(lds + LDS_RING + ((ks + 1) & 15) * 8192 + am[m]);
#pragma unroll
          for (int j = 0; j < 3; ++j) acc[j][m] = mfma(Bw[ks * 3 + j], xa[ks & 1][m], acc[j][m]);
          if (m >= 2 && m < 6) {
            const int mm = (m - 2) >> 1, e0 = ((m - 2) & 1) * 2;
#pragma unroll
            for (int e = e0; e < e0 + 2; ++e) { ssum[mm] = dot2(xa[ks & 1][mm][e], ONES, ssum[mm]); ssq[mm] = dot2(xa[ks & 1][mm][e], xa[ks & 1][mm][e], ssq[mm]); }
          }
          __builtin_amdgcn_sched_barrier(0);
        }
      }
      STAMP(11);
#pragma unroll
      for (int m = 0; m < 2; ++m) {
        float s = sum32(sum16(ssum[m]));
        float q = sum32(sum16(ssq[m]));
        float mean = s * (1.f / 640.f);
        float var = fmaxf(q * (1.f / 640.f) - mean * mean, 0.f);
        float rstd = rsqrtf(var + 1e-5f);
        if (lane < 16) *(f32x2*)(lds + LDS_ST + ((2 * w + m) * 16 + r) * 8) = f32x2{mean, rstd};
      }
      BAR();
      {
        f32x2 st[8];
#pragma unroll
        for (int m = 0; m < 8; ++m) st[m] = *(const f32x2*)(lds + LDS_ST + ((((m + 2 * w) & 7) * 16) + r) * 8);
        SBAR();
#pragma unroll
        for (int j = 0; j < 3; ++j) {
#pragma unroll
          for (int m = 0; m < 8; ++m) {
            const int mt = (m + 2 * w) & 7;
            float g0 = rbf(st[m][1] * (acc[j][m][0] - st[m][0] * c4[j][0])), g1 = rbf(st[m][1] * (acc[j][m][1] - st[m][0] * c4[j][1]));
            float u0 = rbf(st[m][1] * (acc[j][m][2] - st[m][0] * c4[j][2])), u1 = rbf(st[m][1] * (acc[j][m][3] - st[m][0] * c4[j][3]));
            float s0 = __builtin_amdgcn_rcpf(1.f + __builtin_amdgcn_exp2f(-1.4426950408889634f * g0));
            float s1 = __builtin_amdgcn_rcpf(1.f + __builtin_amdgcn_exp2f(-1.4426950408889634f * g1));
            *(unsigned*)(lds + LDS_H + (mt * 16 + r) * HP + (24 * w + 8 * j + 2 * gq) * 2) = pk2(rbf(s0) * u0, rbf(s1) * u1);
          }
        }
      }
      BAR();
      {
        v4u v[6];
#pragma unroll
        for (int i = 0; i < 6; ++i) {
          const int id = tid + 256 * i, mt = (id >> 4) & 7, jc = id >> 7;
          v[i] = *(const v4u*)(lds + LDS_H + (16 * mt + r) * HP + 16 * jc);
        }
        SBAR();
#pragma unroll
        for (int i = 0; i < 6; ++i) {
          const int id = tid + 256 * i, mt = (id >> 4) & 7, jc = id >> 7;
          stc(v[i], rsW, (unsigned)((((mt >> 2) * 48 + 3 * g + (jc >> 2)) * 4 + (mt & 3)) * 1024 + (jc & 3) * 256 + r * 16), LB + L_H + p * 393216);
        }
      }
      publish(fl + PH_H * 256 + p * 16 + g, tid);
      STAMP(12);
      {
        unsigned wb = WD_OFF + (c * 240 + w) * 1024; OPAQUE(wb);
#pragma unroll
        for (int i = 0; i < 60; ++i) Bw[i] = ldb(rsW, l16, wb + i * 4096);
      }
      asm volatile("" ::: "memory");
    }
    {
      FRESH();
      wait_flags(fl + PH_H * 256 + p * 16, 16, 1, w, lane);
      STAMP(13);
      f32x4 acc[5][4];
#pragma unroll
      for (int n = 0; n < 5; ++n)
#pragma unroll
        for (int m = 0; m < 4; ++m) acc[n][m] = f32x4{0, 0, 0, 0};
      unsigned hbase = LB + L_H + p * 393216 + (rh * 48 + w) * 4096; OPAQUE(hbase);
      v4u af[24];
#pragma unroll
      for (int i = 0; i < 6; ++i)
#pragma unroll
        for (int m = 0; m < 4; ++m) af[i * 4 + m] = ldc(rsW, l16 + m * 1024, hbase + i * 16384);
      SBAR();
#pragma unroll
      for (int i = 0; i < 12; ++i) {
#pragma unroll
        for (int m = 0; m < 4; ++m) {
#pragma unroll
          for (int n = 0; n < 5; ++n) acc[n][m] = mfma(Bw[i * 5 + n], af[(i % 6) * 4 + m], acc[n][m]);
          if (i < 6) af[i * 4 + m] = ldc(rsW, l16 + m * 1024, hbase + (i + 6) * 16384);
          SBAR();
        }
      }
      STAMP(14);
#pragma unroll
      for (int n = 0; n < 5; ++n)
#pragma unroll
        for (int m = 0; m < 4; ++m) *(f32x4*)(lds + LDS_RED + (w * 64 + 16 * m + r) * RP + (16 * n + 4 * gq) * 4) = acc[n][m];
      BAR();
      f32x4 rd[3][4][2];
#pragma unroll
      for (int i = 0; i < 3; ++i) {
        const int jc = (i < 2 || w < 2) ? w + 4 * i : w;
#pragma unroll
        for (int q = 0; q < 4; ++q) {
          rd[i][q][0] = *(const f32x4*)(lds + LDS_RED + (q * 64 + lane) * RP + jc * 32);
          rd[i][q][1] = *(const f32x4*)(lds + LDS_RED + (q * 64 + lane) * RP + jc * 32 + 16);
        }
      }
      SBAR();
#pragma unroll
      for (int i = 0; i < 3; ++i) {
        const int jc = w + 4 * i;
        if (jc < 10) {
          f32x4 s0 = (rd[i][0][0] + rd[i][1][0]) + (rd[i][2][0] + rd[i][3][0]), s1 = (rd[i][0][1] + rd[i][1][1]) + (rd[i][2][1] + rd[i][3][1]);
          v4u o;
#pragma unroll
          for (int e = 0; e < 2; ++e) {
            o[e] = pk2(bf2f(res[i][e] & 0xffffu) + rbf(s0[2 * e]), bf2f(res[i][e] >> 16) + rbf(s0[2 * e + 1]));
            o[2 + e] = pk2(bf2f(res[i][2 + e] & 0xffffu) + rbf(s1[2 * e]), bf2f(res[i][2 + e] >> 16) + rbf(s1[2 * e + 1]));
          }
          res[i] = o;
          if (layer == 0) {
            const int c8 = 10 * c + jc;
            stc(o, rsW, (unsigned)((lane >> 4) * 1024 + r * 16), LB + L_X3 + p * 163840 + ((c8 >> 2) * 8 + 4 * rh) * 1024 + (c8 & 3) * 256);
          } else {
            *(v4u*)(out + (size_t)(128 * p + 64 * rh + lane) * 640 + 80 * c + 8 * jc) = o;
          }
        }
      }
      if (layer == 0) {
        publish(fl + PH_X3 * 256 + p * 16 + g, tid);
        unsigned wb = WQ_OFF + (g * 160 + w) * 1024; OPAQUE(wb);
#pragma unroll
        for (int i = 0; i < 40; ++i) Bw[i] = ldb(rsW, l16, wb + i * 4096);
        asm volatile("" ::: "memory");
      }
      STAMP(15);
    }
  }
}

extern "C" void kernel_launch(void* const* d_in, const int* in_sizes, int n_in, void* d_out, int out_size, void* d_ws,
                              size_t ws_size, hipStream_t stream) {
  const u16* x = (const u16*)d_in[0];
  (void)hipFuncSetAttribute((const void*)k_main, hipFuncAttributeMaxDynamicSharedMemorySize, LDS_BYTES);
  k_prep<<<400, 256, 0, stream>>>((const u16*)d_in[1], (const u16*)d_in[2], (const u16*)d_in[4], (const u16*)d_in[5],
                                   (const u16*)d_in[6], (char*)d_ws);
  k_main<<<256, 256, LDS_BYTES, stream>>>(x, (u16*)d_out, (char*)d_ws);
}
```

```cpp
#include <hip/hip_runtime.h>
#include <hip/hip_bf16.h>
#include <stdint.h>
#include <utility>

typedef unsigned short u16;
typedef unsigned v4u __attribute__((ext_vector_type(4)));
typedef unsigned v2u __attribute__((ext_vector_type(2)));
typedef float f32x4 __attribute__((ext_vector_type(4)));
typedef float f32x2 __attribute__((ext_vector_type(2)));
typedef short s16x4 __attribute__((ext_vector_type(4)));
typedef __bf16 bf16x2 __attribute__((ext_vector_type(2)));
typedef __bf16 bf16x8 __attribute__((ext_vector_type(8)));
#define GAS __attribute__((address_space(1)))
#define LAS __attribute__((address_space(3)))
#define DI __device__ __forceinline__
#define RLX_AGENT __ATOMIC_RELAXED, __HIP_MEMORY_SCOPE_AGENT
typedef GAS unsigned long long gu64;

template <int I> struct IC { static constexpr int value = I; };
template <class F, int... I> DI void sfor_(F&& f, std::integer_sequence<int, I...>) { (f(IC<I>{}), ...); }
template <int N, class F> DI void sfor(F&& f) { sfor_(f, std::make_integer_sequence<int, N>{}); }

constexpr int S_ = 2048, D_ = 640, F_ = 1536;
constexpr unsigned FLAG_OFF = 0, FLAG_BYTES = 131072;
constexpr unsigned WQ_OFF = FLAG_BYTES;
constexpr unsigned WO_OFF = WQ_OFF + 16 * 160 * 1024;
constexpr unsigned WG_OFF = WO_OFF + 8 * 100 * 1024;
constexpr unsigned WD_OFF = WG_OFF + 16 * 240 * 1024;
constexpr unsigned CS_OFF = WD_OFF + 8 * 240 * 1024;
constexpr unsigned NULL_OFF = CS_OFF + 20480;
constexpr unsigned STX_OFF = CS_OFF + 32768;
constexpr unsigned ST_BYTES = 2048 * 128;
constexpr unsigned ACT_OFF = STX_OFF + ST_BYTES;
constexpr unsigned KV_BYTES = 16 * 16 * 24 * 1024;
constexpr unsigned XF_BYTES = 16 * 160 * 1024;
constexpr unsigned HF_BYTES = 16 * 384 * 1024;
constexpr unsigned L_KV = 0, L_O = L_KV + KV_BYTES, L_X2 = L_O + XF_BYTES, L_H = L_X2 + XF_BYTES, L_X3 = L_H + HF_BYTES,
                   L_S2 = L_X3 + XF_BYTES, L_S3 = L_S2 + ST_BYTES, LAYER_BYTES = L_S3 + ST_BYTES;
constexpr unsigned WS_USED = ACT_OFF + 2 * LAYER_BYTES;
constexpr unsigned STAMP_OFF = WS_USED;
enum { PH_KV = 0, PH_O = 1, PH_X2 = 2, PH_H = 3, PH_X3 = 4 };

constexpr int TP = 3072;
constexpr int LDS_KVA = 0, LDS_KVB = 12 * TP;
constexpr int LDS_Q = 24 * TP;
constexpr int LDS_R0 = 0;
constexpr int LDS_R1 = 83968;
constexpr int LDS_R1X = LDS_KVB + 8 * TP;
constexpr int LDS_RED = LDS_R1;
constexpr int RP = 336;
constexpr int LDS_H = 0;
constexpr int HP = 208;
constexpr int LDS_ST = LDS_R1 + 19 * 4096;
constexpr int LDS_BYTES = LDS_ST + 1024;

DI float bf2f(unsigned b16) { return __builtin_bit_cast(float, b16 << 16); }
DI unsigned pk2(float a, float b) {
  bf16x2 r; r[0] = (__bf16)a; r[1] = (__bf16)b; return __builtin_bit_cast(unsigned, r);
}
DI float rbf(float a) { return bf2f(pk2(a, 0.f) & 0xffffu); }
template <int SL> DI void mfma_w(f32x4& c, const v4u& xv) { asm volatile("v_mfma_f32_16x16x32_bf16 %0, a[%c2:%c3], %1, %0" : "+v"(c) : "v"(xv), "i"(4 * SL), "i"(4 * SL + 3)); }
template <int SL> DI void mfma_w0(f32x4& c, const v4u& xv) { asm volatile("v_mfma_f32_16x16x32_bf16 %0, a[%c2:%c3], %1, 0" : "=&v"(c) : "v"(xv), "i"(4 * SL), "i"(4 * SL + 3)); }
template <int SL> DI void ldw(__amdgpu_buffer_rsrc_t rs, unsigned voff, unsigned soff) {
  asm volatile("buffer_load_dwordx4 a[%c3:%c4], %0, %1, %2 offen" :: "v"(voff), "s"(rs), "s"(soff), "i"(4 * SL), "i"(4 * SL + 3) : "memory");
}
DI void mfma_v(f32x4& c, const v4u& a, const v4u& b) { asm volatile("s_nop 1\n\tv_mfma_f32_16x16x32_bf16 %0, %1, %2, %0" : "+v"(c) : "v"(a), "v"(b)); }
DI void mfma_v0(f32x4& c, const v4u& a, const v4u& b) { asm volatile("s_nop 1\n\tv_mfma_f32_16x16x32_bf16 %0, %1, %2, 0" : "=&v"(c) : "v"(a), "v"(b)); }
DI void mfma_n(f32x4& c, const v4u& a, const v4u& b) { asm volatile("v_mfma_f32_16x16x32_bf16 %0, %1, %2, %0" : "+v"(c) : "v"(a), "v"(b)); }
DI void mfma_n0(f32x4& c, const v4u& a, const v4u& b) { asm volatile("v_mfma_f32_16x16x32_bf16 %0, %1, %2, 0" : "=&v"(c) : "v"(a), "v"(b)); }
#define MFMA_DRAIN() do { __builtin_amdgcn_sched_barrier(0); asm volatile("s_nop 7\n\ts_nop 7\n\ts_nop 3" ::: "memory"); __builtin_amdgcn_sched_barrier(0); } while (0)
DI float dot2(unsigned a, unsigned b, float c) {
  return __builtin_amdgcn_fdot2_f32_bf16(__builtin_bit_cast(bf16x2, a), __builtin_bit_cast(bf16x2, b), c, false);
}
DI float max3(float a, float b, float c) { float d; asm("v_max3_f32 %0, %1, %2, %3" : "=v"(d) : "v"(a), "v"(b), "v"(c)); return d; }
DI v4u ldb(__amdgpu_buffer_rsrc_t rs, unsigned voff, unsigned soff) { return __builtin_amdgcn_raw_buffer_load_b128(rs, voff, soff, 0); }
DI v4u ldc(__amdgpu_buffer_rsrc_t rs, unsigned voff, unsigned soff) { return __builtin_amdgcn_raw_buffer_load_b128(rs, voff, soff, 16); }
DI void stc(v4u v, __amdgpu_buffer_rsrc_t rs, unsigned voff, unsigned soff) { __builtin_amdgcn_raw_buffer_store_b128(v, rs, voff, soff, 16); }
DI void stc2(v2u v, __amdgpu_buffer_rsrc_t rs, unsigned voff, unsigned soff) { __builtin_amdgcn_raw_buffer_store_b64(v, rs, voff, soff, 16); }
DI void dma(__amdgpu_buffer_rsrc_t rs, unsigned voff, unsigned soff, unsigned lds) {
  unsigned keep;
  asm volatile("s_mov_b32 %0, m0\n\ts_mov_b32 m0, %4\n\ts_nop 0\n\tbuffer_load_dwordx4 %1, %2, %3 offen sc1 lds\n\ts_mov_b32 m0, %0"
               : "=&s"(keep) : "v"(voff), "s"(rs), "s"(soff), "s"(lds) : "memory");
}
template <int LOFF> DI void dma2(__amdgpu_buffer_rsrc_t rs, unsigned voff, unsigned soff, unsigned lbase) {
  asm volatile("s_add_u32 m0, %3, %4\n\ts_nop 0\n\tbuffer_load_dwordx4 %0, %1, %2 offen sc1 lds" :: "v"(voff), "s"(rs), "s"(soff), "s"(lbase), "i"(LOFF) : "memory", "scc");
}
DI float dpp_xor1(float v) { return __builtin_bit_cast(float, __builtin_amdgcn_update_dpp(0, __builtin_bit_cast(int, v), 0xB1, 0xf, 0xf, true)); }
DI float dpp_xor2(float v) { return __builtin_bit_cast(float, __builtin_amdgcn_update_dpp(0, __builtin_bit_cast(int, v), 0x4E, 0xf, 0xf, true)); }
#define OPAQUE(v) asm volatile("" : "+s"(v))
#define FRESH() int tid = tid0, w = w0, p = p0, g = g0; asm volatile("" : "+v"(tid)); asm volatile("" : "+s"(w), "+s"(p), "+s"(g)); const int lane = tid & 63, r = tid & 15, gq = (tid >> 4) & 3, rh = g & 1, c = g >> 1; const unsigned l16 = lane * 16; (void)r; (void)gq; (void)l16; (void)rh; (void)c
template <int N> DI void vmcnt() { asm volatile("s_waitcnt vmcnt(%0)" :: "i"(N > 63 ? 63 : N) : "memory"); }
#define BAR() asm volatile("s_waitcnt lgkmcnt(0)\n\ts_barrier" ::: "memory")
#define SBAR() __builtin_amdgcn_sched_barrier(0)
DI float sum16(float v) { auto t = __builtin_amdgcn_permlane16_swap(__builtin_bit_cast(unsigned, v), __builtin_bit_cast(unsigned, v), false, false);
  return __builtin_bit_cast(float, (unsigned)t[0]) + __builtin_bit_cast(float, (unsigned)t[1]); }
DI float sum32(float v) { auto t = __builtin_amdgcn_permlane32_swap(__builtin_bit_cast(unsigned, v), __builtin_bit_cast(unsigned, v), false, false);
  return __builtin_bit_cast(float, (unsigned)t[0]) + __builtin_bit_cast(float, (unsigned)t[1]); }
DI float max1632(float v) {
  auto t = __builtin_amdgcn_permlane16_swap(__builtin_bit_cast(unsigned, v), __builtin_bit_cast(unsigned, v), false, false);
  float a = __builtin_bit_cast(float, (unsigned)t[0]), b = __builtin_bit_cast(float, (unsigned)t[1]);
  float m = max3(a, b, b);
  auto u = __builtin_amdgcn_permlane32_swap(__builtin_bit_cast(unsigned, m), __builtin_bit_cast(unsigned, m), false, false);
  float c = __builtin_bit_cast(float, (unsigned)u[0]), d = __builtin_bit_cast(float, (unsigned)u[1]);
  return max3(c, d, d);
}
DI float bcast_row1(float v) {
  auto t = __builtin_amdgcn_permlane16_swap(__builtin_bit_cast(unsigned, v), __builtin_bit_cast(unsigned, v), false, false);
  auto u = __builtin_amdgcn_permlane32_swap((unsigned)t[1], (unsigned)t[1], false, false);
  return __builtin_bit_cast(float, (unsigned)u[0]);
}

#define STAMP(i)

constexpr int PREP_QKV = 80, PREP_GU = 160, PREP_WO = 40, PREP_WD = 96, PREP_CS = 78, PREP_ST = 128;
constexpr int PREP_GRID = PREP_QKV + PREP_GU + PREP_WO + PREP_WD + PREP_CS + PREP_ST;
DI int qkv_src_col(int g, int pc) {
  if (pc < 40) return 40 * g + pc;
  if (pc < 60) return 640 + 40 * g + (pc - 40);
  if (pc < 80) return 1280 + 40 * g + (pc - 60);
  if (pc < 100) return 640 + 40 * g + 20 + (pc - 80);
  if (pc < 120) return 1280 + 40 * g + 20 + (pc - 100);
  return -1;
}
__global__ void __launch_bounds__(256) k_prep(const u16* __restrict__ x, const u16* __restrict__ wqkv, const u16* __restrict__ wo,
                                             const u16* __restrict__ wg, const u16* __restrict__ wu, const u16* __restrict__ wd,
                                             char* __restrict__ ws) {
  __shared__ __attribute__((aligned(16))) char sm[32 * 976 + 64];
  const int tid = threadIdx.x, lane = tid & 63, w = tid >> 6;
  int s = blockIdx.x;
  const int gq = lane >> 4, q4 = (lane & 15) >> 2, p4 = lane & 3;
  auto emit = [&](unsigned ldsoff, int pitch, char* dst) {
    const char* a = sm + (8 * gq + q4) * pitch + ldsoff;
    v2u lo = __builtin_bit_cast(v2u, __builtin_amdgcn_ds_read_tr16_b64_v4i16((LAS s16x4*)a));
    v2u hi = __builtin_bit_cast(v2u, __builtin_amdgcn_ds_read_tr16_b64_v4i16((LAS s16x4*)(a + 4 * pitch)));
    *(v4u*)(dst + lane * 16) = v4u{lo[0], lo[1], hi[0], hi[1]};
  };
  if (s < PREP_QKV) {
    const int ks = s >> 2, g0 = (s & 3) * 4;
    constexpr int P = 976;
    v4u v[8];
#pragma unroll
    for (int i = 0; i < 8; ++i) {
      const int id = i < 7 ? tid + 256 * i : (tid < 128 ? tid + 1792 : 1919), row = id / 60, ch = id % 60;
      v[i] = *(const v4u*)(wqkv + (size_t)(32 * ks + row) * 1920 + (ch / 20) * 640 + 40 * g0 + (ch % 20) * 8);
    }
#pragma unroll
    for (int i = 0; i < 8; ++i) {
      const int id = tid + 256 * i, row = id / 60, ch = id % 60;
      if (id < 1920) *(v4u*)(sm + row * P + ch * 16) = v[i];
    }
    if (tid < 32) *(v4u*)(sm + tid * P + 960) = v4u{0, 0, 0, 0};
    __syncthreads();
#pragma unroll
    for (int i = 0; i < 8; ++i) {
      const int sc = qkv_src_col(w, 16 * i + 4 * p4);
      const unsigned off = sc < 0 ? 960 : (sc / 640) * 320 + (sc % 640) * 2;
      emit(off, P, ws + WQ_OFF + (((g0 + w) * 20 + ks) * 8 + (i & 1) * 4 + (i >> 1)) * 1024);
    }
    return;
  }
  s -= PREP_QKV;
  if (s < PREP_GU) {
    const int ks = s >> 3, g0 = (s & 7) * 2;
    constexpr int P = 776;
    v4u v[6];
#pragma unroll
    for (int i = 0; i < 6; ++i) {
      const int id = tid + 256 * i, row = id / 48, ch = id % 48;
      v[i] = *(const v4u*)((ch < 24 ? wg : wu) + (size_t)(32 * ks + row) * 1536 + 96 * g0 + (ch % 24) * 8);
    }
#pragma unroll
    for (int i = 0; i < 6; ++i) {
      const int id = tid + 256 * i, row = id / 48, ch = id % 48;
      char* d = sm + row * P + (ch % 24) * 32 + (ch < 24 ? 0 : 4);
#pragma unroll
      for (int e = 0; e < 4; ++e) *(unsigned*)(d + 8 * e) = v[i][e];
    }
    __syncthreads();
#pragma unroll
    for (int i = 0; i < 6; ++i) {
      const int t = w * 6 + i, gl = t / 12, wv = (t % 12) / 3, j = t % 3;
      emit(t * 32 + p4 * 8, P, ws + WG_OFF + (((g0 + gl) * 20 + ks) * 12 + j * 4 + wv) * 1024);
    }
    return;
  }
  s -= PREP_GU;
  if (s < PREP_WO + PREP_WD) {
    const bool isd = s >= PREP_WO; if (isd) s -= PREP_WO;
    const int ks = s >> 1, hf = s & 1;
    const u16* src = isd ? wd : wo;
    constexpr int P = 648;
    v4u v[5];
#pragma unroll
    for (int i = 0; i < 5; ++i) {
      const int id = tid + 256 * i, row = id / 40, ch = id % 40;
      v[i] = *(const v4u*)(src + (size_t)(32 * ks + row) * 640 + 320 * hf + ch * 8);
    }
#pragma unroll
    for (int i = 0; i < 5; ++i) {
      const int id = tid + 256 * i, row = id / 40, ch = id % 40;
      *(v2u*)(sm + row * P + ch * 16) = v2u{v[i][0], v[i][1]};
      *(v2u*)(sm + row * P + ch * 16 + 8) = v2u{v[i][2], v[i][3]};
    }
    __syncthreads();
#pragma unroll
    for (int i = 0; i < 5; ++i) {
      const int c = 4 * hf + w;
      char* dst = isd ? ws + WD_OFF + (c * 240 + (ks >> 2) * 20 + i * 4 + (ks & 3)) * 1024 : ws + WO_OFF + (c * 100 + (ks >> 2) * 20 + i * 4 + (ks & 3)) * 1024;
      emit((w * 80 + i * 16 + p4 * 4) * 2, P, dst);
    }
    return;
  }
  s -= PREP_WO + PREP_WD;
  if (s < PREP_CS) {
    const int cp = tid & 7, rg = tid >> 3;
    const u16* src; int ld, col, o;
    if (s < 30) { src = wqkv; ld = 1920; col = s * 64; o = col; }
    else if (s < 54) { src = wg; ld = 1536; col = (s - 30) * 64; o = 1920 + col; }
    else { src = wu; ld = 1536; col = (s - 54) * 64; o = 3456 + col; }
    v4u v[20];
#pragma unroll
    for (int i = 0; i < 20; ++i) v[i] = *(const v4u*)(src + (size_t)(rg + 32 * i) * ld + col + 8 * cp);
    float a[8] = {0, 0, 0, 0, 0, 0, 0, 0};
#pragma unroll
    for (int i = 0; i < 20; ++i)
#pragma unroll
      for (int e = 0; e < 4; ++e) { a[2 * e] += bf2f(v[i][e] & 0xffffu); a[2 * e + 1] += bf2f(v[i][e] >> 16); }
    *(f32x4*)(sm + (rg * 64 + cp * 8) * 4) = f32x4{a[0], a[1], a[2], a[3]};
    *(f32x4*)(sm + (rg * 64 + cp * 8 + 4) * 4) = f32x4{a[4], a[5], a[6], a[7]};
    __syncthreads();
    if (tid < 64) {
      float t = 0.f;
#pragma unroll
      for (int k = 0; k < 32; ++k) t += ((float*)sm)[k * 64 + tid];
      ((float*)(ws + CS_OFF))[o + tid] = t;
    }
    return;
  }
  s -= PREP_CS;
  {
    if (s < 32) ((v4u*)(ws + FLAG_OFF))[s * 256 + tid] = v4u{0, 0, 0, 0};
    else if (s < 35) { const int ch = (s - 32) * 256 + tid, o = (ch * 16) % 3072; ((v4u*)(ws + NULL_OFF))[ch] = v4u{o == 2560 ? 0x3f80u : (o == 2576 ? 0xF14Au : 0u), 0, 0, 0}; }
    const int row = s * 16 + w * 4 + gq, r = lane & 15;
    float sm1 = 0.f, sq = 0.f;
#pragma unroll
    for (int i = 0; i < 5; ++i) {
      const v4u v = ((const v4u*)(x + (size_t)row * 640))[r + 16 * i];
#pragma unroll
      for (int e = 0; e < 4; ++e) { sm1 = dot2(v[e], 0x3f803f80u, sm1); sq = dot2(v[e], v[e], sq); }
    }
#pragma unroll
    for (int o = 8; o >= 1; o >>= 1) { sm1 += __shfl_xor(sm1, o); sq += __shfl_xor(sq, o); }
    ((f32x2*)(ws + STX_OFF))[row * 16 + r] = r == 0 ? f32x2{sm1, sq} : f32x2{0.f, 0.f};
  }
}

struct Sched { int op[20][4][2]; };
constexpr void put(Sched& s, int ks, int m, int code) { if (s.op[ks][m][0] == 0) s.op[ks][m][0] = code; else s.op[ks][m][1] = code; }
constexpr int weight_of(int c) { return c == 0 ? 0 : (c >= 4000 ? 0 : (c == 3003 ? 2 : (((c >= 1000 && c < 3000) || c == 3001) ? 1 : 0))); }
constexpr int ops_before(const Sched& s, int ks_end, int m_end = 0) {
  int n = 0;
  for (int ks = 0; ks < 20; ++ks) for (int m = 0; m < 4; ++m) { if (ks > ks_end || (ks == ks_end && m >= m_end)) return n; n += weight_of(s.op[ks][m][0]) + weight_of(s.op[ks][m][1]); }
  return n;
}
constexpr int ops_after_code(const Sched& s, int code, int ks_end, int m_end = 0) {
  int n = 0; bool seen = false;
  for (int ks = 0; ks < 20; ++ks) for (int m = 0; m < 4; ++m) {
    if (ks > ks_end || (ks == ks_end && m >= m_end)) return n;
    for (int e = 0; e < 2; ++e) { if (seen) n += weight_of(s.op[ks][m][e]); if (s.op[ks][m][e] == code) seen = true; }
  }
  return n;
}
constexpr void put_stats(Sched& s, int ks0) { for (int i = 0; i < 6; ++i) put(s, ks0 + i / 2, (i & 1) * 2, 3010 + i); }
constexpr Sched mk_s1() {
  Sched s{};
  for (int i = 0; i < 20; ++i) put(s, i / 2, (i & 1) * 2 + 1, 2000 + i);
  put_stats(s, 6); put(s, 12, 0, 3005); put(s, 12, 1, 3001); put(s, 16, 0, 3002); put(s, 16, 1, 3003);
  for (int i = 0; i < 7; ++i) put(s, 16 + (i + 2) / 4 + (i >= 6), i < 6 ? (i + 2) % 4 : 3, 1000 + i);
  put(s, 18, 0, 3030);
  return s;
}
constexpr Sched mk_s2() {
  Sched s{};
  put(s, 0, 0, 3020); put(s, 1, 0, 3021); put(s, 1, 2, 3022); put(s, 2, 0, 3023);
  for (int i = 0; i < 35; ++i) put(s, 1 + i / 2, (i & 1) * 2 + 1, 2000 + i);
  put_stats(s, 5); put(s, 9, 0, 3000); put(s, 15, 0, 3001); put(s, 18, 2, 3002);
  put(s, 18, 2, 4000); put(s, 18, 3, 4001); put(s, 19, 0, 4002); put(s, 19, 1, 4003); put(s, 19, 2, 4004); put(s, 19, 3, 4005);
  put(s, 18, 0, 3030);
  return s;
}
constexpr int GLATE0 = 12;
constexpr Sched mk_s8() {
  Sched s{};
  for (int i = GLATE0; i < 25; ++i) put(s, 1 + (i - GLATE0) / 2, ((i - GLATE0) & 1) * 2 + 1, 2000 + 35 + i);
  put_stats(s, 5); put(s, 8, 0, 3001); put(s, 11, 0, 3002); put(s, 11, 0, 3005); put(s, 11, 1, 3003);
  for (int i = 0; i < 19; ++i) put(s, 11 + (i + 2) / 3, 1 + (i + 2) % 3, 1000 + i);
  put(s, 18, 0, 3030);
  return s;
}
constexpr int NBD9 = 46;
constexpr Sched mk_s9() {
  Sched s{};
  put(s, 0, 0, 3020); put(s, 0, 2, 3021); put(s, 0, 3, 3022); put(s, 1, 0, 3023);
  int f = 0;
  for (int ks = 1; ks < 20; ++ks) for (int m = 1; m < 4; ++m) { if (ks >= 12 && m == 2) continue; if (f < NBD9 && f < 3 * ks) put(s, ks, m, 2000 + f++); }
  put_stats(s, 3); put(s, 6, 0, 3000); put(s, 13, 0, 3001); put(s, 16, 0, 3002);
  for (int i = 0; i < 8; ++i) put(s, 16 + i / 2, 2, 5000 + i);
  put(s, 18, 0, 3030);
  return s;
}
constexpr Sched SS1 = mk_s1(), SS2 = mk_s2(), SS8 = mk_s8(), SS9 = mk_s9();
constexpr int count_w(const Sched& s) { int n = 0; for (int ks = 0; ks < 20; ++ks) for (int m = 0; m < 4; ++m) for (int e = 0; e < 2; ++e) if (s.op[ks][m][e] >= 2000 && s.op[ks][m][e] < 3000) ++n; return n; }
static_assert(count_w(SS9) == NBD9, "down fragments in P4(b)");

DI unsigned poll_issue(__amdgpu_buffer_rsrc_t rs, unsigned voff, unsigned soff) {
  unsigned v; asm volatile("buffer_load_dword %0, %1, %2, %3 offen sc1" : "=&v"(v) : "v"(voff), "s"(rs), "s"(soff) : "memory"); return v;
}
template <int N> DI void poll_check(unsigned v, __amdgpu_buffer_rsrc_t rs, unsigned voff, unsigned soff, bool need = true) {
  asm volatile("s_waitcnt vmcnt(%1)" : "+v"(v) : "i"(N > 63 ? 63 : N) : "memory");
  if (need)
#pragma clang loop unroll(disable)
    for (int spins = 0; spins < (1 << 22) && !__all(v == 1u); ++spins)
      asm volatile("s_sleep 2\n\tbuffer_load_dword %0, %1, %2, %3 offen sc1\n\ts_waitcnt vmcnt(0)" : "=&v"(v) : "v"(voff), "s"(rs), "s"(soff) : "memory");
  asm volatile("" ::: "memory");
}
template <int N> DI void set_flag(__amdgpu_buffer_rsrc_t rs, unsigned soff) {
  vmcnt<N>();
  __builtin_amdgcn_raw_buffer_store_b32(1u, rs, 0, soff, 16);
  asm volatile("" ::: "memory");
}
DI v4u ldc_asm(__amdgpu_buffer_rsrc_t rs, unsigned voff, unsigned soff) {
  v4u v; asm volatile("buffer_load_dwordx4 %0, %1, %2, %3 offen sc1" : "=&v"(v) : "v"(voff), "s"(rs), "s"(soff) : "memory"); return v;
}
template <int F> DI unsigned rot5(unsigned base, int h) { constexpr int i = F / 5, n = F % 5; return base + (unsigned)(i * 5 + n) * 4096u + (h ? (unsigned)(((n + 3) % 5) - n) * 4096u : 0u); }
DI unsigned flag_off(int layer, int ph, int p, int sub) { return FLAG_OFF + (unsigned)(((layer * 5 + ph) * 16 + p) * 2 + sub) * 256; }

__global__ void __launch_bounds__(256, 1) k_main(const u16* __restrict__ x, u16* __restrict__ out, char* __restrict__ ws) {
  extern __shared__ __attribute__((aligned(16))) char lds[];
  const int tid0 = threadIdx.x;
  const int w0 = __builtin_amdgcn_readfirstlane(tid0 >> 6);
  const int b = blockIdx.x, x8 = b & 7, rr = b >> 3;
  const int p0 = (x8 >> 1) * 4 + (rr >> 3), g0 = (x8 & 1) * 8 + (rr & 7);
  const __amdgpu_buffer_rsrc_t rsW = __builtin_amdgcn_make_buffer_rsrc((void*)ws, 0, 0x7fffffff, 0x00020000);
  const __amdgpu_buffer_rsrc_t rsX = __builtin_amdgcn_make_buffer_rsrc((void*)x, 0, S_ * D_ * 2, 0x00020000);

  asm volatile("" ::: "a0", "a1", "a2", "a3", "a4", "a5", "a6", "a7", "a8", "a9", "a10", "a11", "a12", "a13", "a14", "a15", "a16", "a17", "a18", "a19", "a20", "a21", "a22", "a23", "a24", "a25", "a26", "a27", "a28", "a29", "a30", "a31", "a32", "a33", "a34", "a35", "a36", "a37", "a38", "a39", "a40", "a41", "a42", "a43", "a44", "a45", "a46", "a47", "a48", "a49", "a50", "a51", "a52", "a53", "a54", "a55", "a56", "a57", "a58", "a59", "a60", "a61", "a62", "a63", "a64", "a65", "a66", "a67", "a68", "a69", "a70", "a71", "a72", "a73", "a74", "a75", "a76", "a77", "a78", "a79", "a80", "a81", "a82", "a83", "a84", "a85", "a86", "a87", "a88", "a89", "a90", "a91", "a92", "a93", "a94", "a95", "a96", "a97", "a98", "a99", "a100", "a101", "a102", "a103", "a104", "a105", "a106", "a107", "a108", "a109", "a110", "a111", "a112", "a113", "a114", "a115", "a116", "a117", "a118", "a119", "a120", "a121", "a122", "a123", "a124", "a125", "a126", "a127", "a128", "a129", "a130", "a131", "a132", "a133", "a134", "a135", "a136", "a137", "a138", "a139", "a140", "a141", "a142", "a143", "a144", "a145", "a146", "a147", "a148", "a149", "a150", "a151", "a152", "a153", "a154", "a155", "a156", "a157", "a158", "a159", "a160", "a161", "a162", "a163", "a164", "a165", "a166", "a167", "a168", "a169", "a170", "a171", "a172", "a173", "a174", "a175", "a176", "a177", "a178", "a179", "a180", "a181", "a182", "a183", "a184", "a185", "a186", "a187", "a188", "a189", "a190", "a191", "a192", "a193", "a194", "a195", "a196", "a197", "a198", "a199", "a200", "a201", "a202", "a203", "a204", "a205", "a206", "a207", "a208", "a209", "a210", "a211", "a212", "a213", "a214", "a215", "a216", "a217", "a218", "a219", "a220", "a221", "a222", "a223", "a224", "a225", "a226", "a227", "a228", "a229", "a230", "a231", "a232", "a233", "a234", "a235", "a236", "a237", "a238", "a239");
  v2u res[2][3];
  v4u st0, st1;
  {
    FRESH();
    unsigned wb = WQ_OFF + (g * 160 + w) * 1024; OPAQUE(wb);
    st0 = ldc_asm(rsW, tid * 32, STX_OFF + p * 16384); st1 = ldc_asm(rsW, tid * 32 + 16, STX_OFF + p * 16384);
    sfor<8>([&](auto I) { ldw<I.value>(rsW, l16, wb + I.value * 4096); });
    unsigned ab = (128 * p + 16 * w) * 1280, lb = LDS_R0 + w * 1024; OPAQUE(ab); OPAQUE(lb);
    const unsigned avoff = r * 1280 + gq * 16;
    sfor<4>([&](auto I) { dma2<I.value * 4096>(rsX, avoff, ab + I.value * 64, lb); });
    sfor<32>([&](auto I) { ldw<8 + I.value>(rsW, l16, wb + (8 + I.value) * 4096); });
#pragma unroll
    for (int s = 0; s < 2; ++s)
#pragma unroll
      for (int i = 0; i < 3; ++i)
        res[s][i] = __builtin_amdgcn_raw_buffer_load_b64(rsX, (unsigned)(r * 1280 + gq * 8),
                                                         (unsigned)((128 * p + 64 * s + 32 * rh + 16 * (w & 1)) * 1280 + 160 * c + 32 * ((i + 3 * (w >> 1)) % 5)), 0);
    sfor<16>([&](auto I) { dma2<(4 + I.value) * 4096>(rsX, avoff, ab + (4 + I.value) * 64, lb); });
  }

  float sm_, sq_;
  auto stats_stage = [&](auto STc, int sub, int tid) {
    constexpr int ST = STc.value;
    if constexpr (ST == 0) {
      asm volatile("" : "+v"(st0), "+v"(st1));
      f32x4 fa = __builtin_bit_cast(f32x4, st0) + __builtin_bit_cast(f32x4, st1);
      sm_ = fa[0] + fa[2]; sq_ = fa[1] + fa[3];
    } else if constexpr (ST == 1) { sm_ += dpp_xor1(sm_); sq_ += dpp_xor1(sq_); }
    else if constexpr (ST == 2) { sm_ += dpp_xor2(sm_); sq_ += dpp_xor2(sq_); }
    else if constexpr (ST == 3) { sm_ *= (1.f / 640.f); sq_ = fmaxf(sq_ * (1.f / 640.f) - sm_ * sm_, 0.f); sq_ = rbf(sq_); }
    else if constexpr (ST == 4) { sq_ = rbf(__builtin_amdgcn_sqrtf(rbf(sq_ + 1e-5f))); }
    else { if ((tid & 3) == 0) *(f32x2*)(lds + LDS_ST + (sub * 64 + (tid >> 2)) * 8) = f32x2{rbf(sm_), __builtin_amdgcn_rcpf(sq_)}; }
  };
  auto ring_gemm = [&](auto NJc, auto SUBc, auto NAFTERc, const Sched& dummy, auto SCH, f32x4 (&acc)[3][4], unsigned l16, auto&& bg) {
    constexpr int NJ = NJc.value, SUB = SUBc.value, NAFTER = NAFTERc.value;
    constexpr const Sched& S = SCH.get();
    unsigned rb0 = (SUB == 0 ? LDS_R0 : LDS_R1) + l16, rb1 = rb0 + 65536, rbx = LDS_R1X + l16;
    asm volatile("" : "+v"(rb0), "+v"(rb1), "+v"(rbx));
    auto rd = [&](int ks, int m) -> v4u {
      if (SUB == 1 && ks == 19) return *(const LAS v4u*)(rbx + m * 1024);
      return ks < 16 ? *(const LAS v4u*)(rb0 + ks * 4096 + m * 1024) : *(const LAS v4u*)(rb1 + (ks - 16) * 4096 + m * 1024);
    };
    auto gend = [](int k) constexpr -> int {
      if (SUB == 0) return k == 0 ? 1 : (k == 2 ? 3 : ((k % 4 == 0) ? k + 3 : -1));
      return k == 0 ? 9 : (k == 10 ? 19 : -1);
    };
    v4u xa[3][4];
    vmcnt<19 - gend(0) + NAFTER>();
    BAR();
#pragma unroll
    for (int k = 0; k < 2; ++k)
#pragma unroll
      for (int m = 0; m < 4; ++m) xa[k][m] = rd(k, m);
    SBAR();
    sfor<20>([&](auto KS) {
      constexpr int ks = KS.value;
      if constexpr (ks + 2 < 20 && gend(ks + 2) >= 0) { vmcnt<19 - gend(ks + 2) + NAFTER + ops_before(S, ks)>(); asm volatile("s_barrier" ::: "memory"); }
      if constexpr (ks == 18) BAR();
      sfor<4>([&](auto M) {
        constexpr int m = M.value;
        sfor<NJ>([&](auto J) {
          constexpr int j = J.value;
          if constexpr (ks == 0) mfma_w0<j>(acc[j][m], xa[0][m]); else mfma_w<ks * NJ + j>(acc[j][m], xa[ks % 3][m]);
          if constexpr (j == 0 && ks < 18) xa[(ks + 2) % 3][m] = rd(ks + 2, m);
          if constexpr (j == 1 && S.op[ks][m][0] != 0) bg(IC<S.op[ks][m][0]>{}, KS, M);
          if constexpr (j == NJ - 1 && S.op[ks][m][1] != 0) bg(IC<S.op[ks][m][1]>{}, KS, M);
          SBAR();
        });
      });
    });
    MFMA_DRAIN();
  };

#pragma unroll 1
  for (int layer = 0; layer < 2; ++layer) {
    const unsigned LB = ACT_OFF + layer * LAYER_BYTES;
    {
      FRESH();
      STAMP(0);
      f32x4 c4[2];
#pragma unroll
      for (int j = 0; j < 2; ++j) {
        const int sc = qkv_src_col(g, 16 * (2 * w + j) + 4 * gq);
        c4[j] = __builtin_bit_cast(f32x4, ldb(rsW, (unsigned)(sc < 0 ? 0 : sc) * 4, CS_OFF));
      }
      asm volatile("" ::: "memory");
      __amdgpu_buffer_rsrc_t rsA = layer == 0 ? rsX : rsW;
      unsigned avoff, abase, sb, stb;
      if (layer == 0) { avoff = r * 1280 + gq * 16; abase = (128 * p + 64 + 16 * w) * 1280; sb = 64; stb = STX_OFF + p * 16384 + 8192; }
      else { avoff = l16; abase = ACT_OFF + L_X3 + p * 163840 + 81920 + w * 1024; sb = 4096; stb = ACT_OFF + L_S3 + p * 16384 + 8192; }
      unsigned lb1 = LDS_R1 + w * 1024, wob = WO_OFF + (c * 100 + w) * 1024, wgb = WG_OFF + (g * 240 + w) * 1024;
      OPAQUE(abase); OPAQUE(lb1); OPAQUE(sb); OPAQUE(wob); OPAQUE(wgb);
      f32x4 acc[3][4];
      unsigned fv = 1u;
      f32x2 st[4]; v4u sv[3];
      auto epilogue1 = [&](auto SUBc, auto&& mid) {
        constexpr int SUB = SUBc.value;
        constexpr int KVO = (SUB == 0 ? LDS_KVA : LDS_KVB) + 8 * TP;
        if (tid < 8) *(v4u*)(lds + KVO + (tid >> 1) * TP + 2560 + (tid & 1) * 16) = v4u{(tid & 1) ? 0u : 0x00003f80u, 0, 0, 0};
        mid(IC<0>{});
        SBAR();
        sfor<2>([&](auto J) {
          constexpr int j = J.value;
          const int pc0 = 16 * (2 * w + j) + 4 * gq;
          const int a0 = pc0 < 40 ? LDS_Q + (SUB * 64 + r) * 80 + pc0 * 2 : (pc0 < 120 ? KVO + r * 160 + (pc0 - 40) * 2 : KVO + 2688 + r * 8);
          const int ts = pc0 < 40 ? 1280 : TP;
          const f32x2 ca = {c4[j][0], c4[j][1]}, cb = {c4[j][2], c4[j][3]};
          sfor<4>([&](auto M) {
            constexpr int m = M.value;
            const f32x2 nm = {-st[m][0], -st[m][0]}, rs2 = {st[m][1], st[m][1]};
            f32x2 ya = __builtin_elementwise_fma(nm, ca, f32x2{acc[j][m][0], acc[j][m][1]}) * rs2;
            f32x2 yb = __builtin_elementwise_fma(nm, cb, f32x2{acc[j][m][2], acc[j][m][3]}) * rs2;
            *(v2u*)(lds + a0 + m * ts) = v2u{pk2(ya[0], ya[1]), pk2(yb[0], yb[1])};
          });
          sfor<4>([&](auto M) { mid(IC<1 + j * 4 + M.value>{}); });
          SBAR();
        });
      };
      struct G1 { static constexpr const Sched& get() { return SS1; } };
      ring_gemm(IC<2>{}, IC<0>{}, IC<0>{}, SS1, G1{}, acc, l16, [&](auto OP, auto KS, auto M) {
        constexpr int op = OP.value;
        if constexpr (op >= 2000 && op < 3000) ldw<40 + op - 2000>(rsW, l16, rot5<op - 2000>(wob, w >> 1));
        else if constexpr (op >= 1000 && op < 2000) dma2<(op - 1000) * 4096>(rsA, avoff, abase + (op - 1000) * sb, lb1);
        else if constexpr (op == 3001) fv = poll_issue(rsW, lane * 4, flag_off(0, PH_X3, p, 1));
        else if constexpr (op == 3002) poll_check<ops_after_code(SS1, 3001, KS.value, M.value)>(fv, rsW, lane * 4, flag_off(0, PH_X3, p, 1), layer > 0);
        else if constexpr (op == 3005) asm volatile("" : "+v"(c4[0]), "+v"(c4[1]));
        else if constexpr (op == 3003) { st0 = ldc_asm(rsW, tid * 32, stb); st1 = ldc_asm(rsW, tid * 32 + 16, stb); }
        else if constexpr (op >= 3010 && op < 3016) stats_stage(IC<op - 3010>{}, 0, tid);
        else if constexpr (op == 3030) {
#pragma unroll
          for (int m = 0; m < 4; ++m) st[m] = *(const f32x2*)(lds + LDS_ST + (m * 16 + r) * 8);
        }
      });
      STAMP(1);
      epilogue1(IC<0>{}, [&](auto I) {
        constexpr int i = I.value;
        if constexpr (i == 0) {
        } else if constexpr (i <= 6) { dma2<(5 + 2 * i) * 4096>(rsA, avoff, abase + (5 + 2 * i) * sb, lb1); dma2<(6 + 2 * i) * 4096>(rsA, avoff, abase + (6 + 2 * i) * sb, lb1); }
        else if constexpr (i == 7) dma2<LDS_R1X - LDS_R1>(rsA, avoff, abase + 19 * sb, lb1);
      });
      STAMP(2);
      const unsigned kvf_v = (unsigned)((((lane >> 2) & 1) && p >= 2 ? 512 : 0) + (g * 4 + (lane & 3)) * 4);
      const unsigned kvf_s = flag_off(layer, PH_KV, p >= 2 ? p - 2 : 0, 0);
      unsigned kb1 = (p >= 1 ? LB + L_KV + ((p - 1) * 16 + g) * 24576 : NULL_OFF) + w * 1024, kb2 = (p >= 2 ? LB + L_KV + ((p - 2) * 16 + g) * 24576 : NULL_OFF) + w * 1024, lkb = w * 1024;
      OPAQUE(kb1); OPAQUE(kb2); OPAQUE(lkb);
      struct G2 { static constexpr const Sched& get() { return SS2; } };
      ring_gemm(IC<2>{}, IC<1>{}, IC<0>{}, SS2, G2{}, acc, l16, [&](auto OP, auto KS, auto M) {
        constexpr int op = OP.value;
        if constexpr (op >= 2000 && op < 3000) ldw<op - 2000>(rsW, l16, wgb + (op - 2000) * 4096);
        else if constexpr (op == 3000) set_flag<ops_after_code(SS2, 3023, KS.value, M.value)>(rsW, flag_off(layer, PH_KV, p, 0) + (g * 4 + w) * 4);
        else if constexpr (op == 3001) fv = poll_issue(rsW, kvf_v, kvf_s);
        else if constexpr (op == 3002) poll_check<ops_after_code(SS2, 3001, KS.value, M.value)>(fv, rsW, kvf_v, kvf_s, p > 0);
        else if constexpr (op >= 4000 && op < 4003) dma2<LDS_KVA + 4 * TP + (op - 4000) * 4096>(rsW, l16, kb1 + (op - 4000) * 4096, lkb);
        else if constexpr (op >= 4003 && op < 4006) dma2<LDS_KVA + (op - 4003) * 4096>(rsW, l16, kb2 + (op - 4003) * 4096, lkb);
        else if constexpr (op >= 3010 && op < 3016) stats_stage(IC<op - 3010>{}, 1, tid);
        else if constexpr (op == 3020) {
#pragma unroll
          for (int i = 0; i < 3; ++i) sv[i] = *(const v4u*)(lds + LDS_KVA + 8 * TP + (tid + 256 * i) * 16);
        } else if constexpr (op > 3020 && op <= 3023) { stc(sv[op - 3021], rsW, (unsigned)(tid + 256 * (op - 3021)) * 16, LB + L_KV + (p * 16 + g) * 24576); asm volatile("" ::: "memory"); }
        else if constexpr (op == 3030) {
#pragma unroll
          for (int m = 0; m < 4; ++m) st[m] = *(const f32x2*)(lds + LDS_ST + (64 + m * 16 + r) * 8);
        }
      });
      STAMP(3);
      epilogue1(IC<1>{}, [&](auto) {});
      STAMP(4);
      vmcnt<0>();
      BAR();
    }
    v4u af[24];
    {
      FRESH();
      unsigned wob = WO_OFF + (c * 100 + w) * 1024; OPAQUE(wob);
      const unsigned kvf_v = (unsigned)((((lane >> 2) & 1) && p >= 2 ? 512 : 0) + (g * 4 + (lane & 3)) * 4);
      const unsigned kvf_s = flag_off(layer, PH_KV, p >= 2 ? p - 2 : 0, 1);
      unsigned kb1 = (p >= 1 ? LB + L_KV + ((p - 1) * 16 + g) * 24576 + 12288 : NULL_OFF) + w * 1024, kb2 = (p >= 2 ? LB + L_KV + ((p - 2) * 16 + g) * 24576 + 12288 : NULL_OFF) + w * 1024, lkb = w * 1024;
      OPAQUE(kb1); OPAQUE(kb2); OPAQUE(lkb);
      const int hh = w & 1, jb = (w >> 1) * 2;
      const float SC = 0.22360679774997896f * 1.4426950408889634f;
      const int q4 = (lane & 15) >> 2, p4 = lane & 3;
      const unsigned koff = gq < 3 ? r * 160 + hh * 80 + 16 * gq : 2576;
      const unsigned voff0 = (4 * gq + q4) * 160 + hh * 80 + 40 + 8 * p4;
      const unsigned voff1 = p4 == 0 ? (4 * gq + q4) * 160 + hh * 80 + 40 + 32 : (p4 == 1 ? 2560 : 2568);
      bool up[4];
#pragma unroll
      for (int e = 0; e < 4; ++e) up[e] = (4 * gq + e) > r;
      const unsigned mA0 = (up[0] ? 0xffffu : 0u) | (up[1] ? 0xffff0000u : 0u), mA1 = (up[2] ? 0xffffu : 0u) | (up[3] ? 0xffff0000u : 0u);
      f32x4 o0[4], o1[4]; float mx[4];
      unsigned fv = 1u;
      v4u kf[10]; v4u qf[2]; v2u qlo[2], qhi[2];
      auto dbase = [&](int D) -> const char* { return lds + (D < 2 ? LDS_KVA : LDS_KVB) + ((D == 0 || D == 3) ? jb : (D == 1 ? 4 : 0)) * TP; };
      auto qrow_of = [&](int D, int u) -> char* { return lds + LDS_Q + (16 * ((D & 1) * 4 + jb + u) + r) * 80 + hh * 40; };
      auto read_q = [&](int D) {
#pragma unroll
        for (int u = 0; u < 2; ++u) {
          qlo[u] = *(const v2u*)(qrow_of(D, u) + (gq < 2 ? 16 * gq : 32));
          qhi[u] = *(const v2u*)(qrow_of(D, u) + (gq < 2 ? 16 * gq + 8 : 32));
        }
      };
      auto make_q = [&]() {
#pragma unroll
        for (int u = 0; u < 2; ++u) qf[u] = v4u{gq < 3 ? qlo[u][0] : 0x3f80u, gq < 3 ? qlo[u][1] : 0u, gq < 2 ? qhi[u][0] : 0u, gq < 2 ? qhi[u][1] : 0u};
        asm volatile("" : "+v"(qf[0]), "+v"(qf[1]));
      };
      auto read_k = [&](int D, int t) { kf[t] = *(const v4u*)(dbase(D) + t * TP + koff); };
      auto load_kq = [&](auto Dc) {
        constexpr int D = Dc.value;
        read_q(D);
#pragma unroll
        for (int t = 0; t < ((D == 0 || D == 3) ? 10 : 8); ++t) read_k(D, t);
        make_q();
        SBAR();
      };
      auto dchunk = [&](auto Dc, auto PIPEc, auto&& bg) {
        constexpr int D = Dc.value, UB = (D & 1) * 2; constexpr bool MASKED = D == 0 || D == 3, FIRST = D < 2, PIPE = PIPEc.value != 0;
        constexpr int NT = MASKED ? 9 : 8, NP = MASKED ? 5 : 4, NKN = (D + 1 == 3) ? 10 : 8;
        f32x4 s[2][9]; v4u vq0[5], vq1[5];
        const char* kvb = dbase(D);
        auto read_v = [&](int pr, int hf) {
          const v2u a = __builtin_bit_cast(v2u, __builtin_amdgcn_ds_read_tr16_b64_v4i16((LAS s16x4*)(kvb + 2 * pr * TP + (hf ? voff1 : voff0))));
          const v2u bq = __builtin_bit_cast(v2u, __builtin_amdgcn_ds_read_tr16_b64_v4i16((LAS s16x4*)(kvb + (2 * pr + 1) * TP + (hf ? voff1 : voff0))));
          (hf ? vq1 : vq0)[pr] = v4u{a[0], a[1], bq[0], bq[1]};
        };
        float ma[2], mb[2];
        auto tord = [](int i) constexpr -> int { return MASKED ? (i == 0 ? 0 : (i == 1 ? 8 : i - 1)) : i; };
        auto maxstep = [&](auto Ic) {
          constexpr int i = Ic.value;
#pragma unroll
          for (int u = 0; u < 2; ++u) {
            if constexpr (MASKED && i == 0) {}
            else if constexpr (MASKED && i == 1) {
#pragma unroll
              for (int e = 0; e < 4; ++e) s[u][0][e] = up[e] ? s[u][0][e] : s[u][8][e];
              ma[u] = max3(s[u][0][0], s[u][0][1], s[u][0][1]); mb[u] = max3(s[u][0][2], s[u][0][3], s[u][0][3]);
            } else if constexpr (i == 0) { ma[u] = max3(s[u][0][0], s[u][0][1], s[u][0][1]); mb[u] = max3(s[u][0][2], s[u][0][3], s[u][0][3]); }
            else { constexpr int t = tord(i); ma[u] = max3(ma[u], s[u][t][0], s[u][t][1]); mb[u] = max3(mb[u], s[u][t][2], s[u][t][3]); }
          }
        };
        asm volatile("s_nop 1" ::: "memory");
        sfor<NT>([&](auto I) {
          constexpr int i = I.value, t = tord(i);
          mfma_n0(s[0][t], kf[t], qf[0]);
          if constexpr (i < 2 * NP) read_v(i >> 1, i & 1);
          SBAR();
          mfma_n0(s[1][t], kf[MASKED ? t + 1 : t], qf[1]);
          if constexpr (i >= 2) maxstep(IC<i - 2>{});
          SBAR();
        });
        if constexpr (MASKED) read_v(4, 1);
        asm volatile("s_nop 7" ::: "memory");
        SBAR();
        maxstep(IC<NT - 2>{});
        asm volatile("s_nop 3" ::: "memory");
        SBAR();
        maxstep(IC<NT - 1>{});
        SBAR();
        bg(IC<0>{});
        if constexpr (PIPE) read_q(D + 1);
        float mc[2];
#pragma unroll
        for (int u = 0; u < 2; ++u) {
          const float m = max1632(FIRST ? max3(ma[u], mb[u], mb[u]) : max3(ma[u], mb[u], mx[UB + u]));
          mc[u] = -m * SC;
          if constexpr (!FIRST) {
            const float al = __builtin_amdgcn_exp2f(fmaf(mx[UB + u], SC, mc[u]));
            o0[UB + u] *= al; o1[UB + u] *= al;
          }
          mx[UB + u] = m;
        }
        const f32x2 sc2 = {SC, SC};
#pragma unroll
        for (int t = 0; t < 8; ++t)
#pragma unroll
          for (int u = 0; u < 2; ++u) {
            const f32x2 za = __builtin_elementwise_fma(f32x2{s[u][t][0], s[u][t][1]}, sc2, f32x2{mc[u], mc[u]});
            const f32x2 zb = __builtin_elementwise_fma(f32x2{s[u][t][2], s[u][t][3]}, sc2, f32x2{mc[u], mc[u]});
            s[u][t] = f32x4{za[0], za[1], zb[0], zb[1]};
          }
        SBAR();
        unsigned pp[2][8][2], pe[2][4];
        auto pv = [&](auto PRc, auto Uc, auto Hc, auto F0c) {
          constexpr int pr = PRc.value, u = Uc.value, hsel = Hc.value;
          v4u pf;
          if constexpr (!MASKED) pf = v4u{pp[u][2 * pr][0], pp[u][2 * pr][1], pp[u][2 * pr + 1][0], pp[u][2 * pr + 1][1]};
          else if constexpr (u == 0) {
            if constexpr (pr == 0) pf = v4u{pe[0][0], pe[0][1], pp[0][1][0], pp[0][1][1]};
            else if constexpr (pr == 4) pf = v4u{pe[0][2], pe[0][3], 0u, 0u};
            else pf = v4u{pp[0][2 * pr][0], pp[0][2 * pr][1], pp[0][2 * pr + 1][0], pp[0][2 * pr + 1][1]};
          } else {
            if constexpr (pr == 0) pf = v4u{0u, 0u, pe[1][0], pe[1][1]};
            else if constexpr (pr == 4) pf = v4u{pp[1][7][0], pp[1][7][1], pe[1][2], pe[1][3]};
            else pf = v4u{pp[1][2 * pr - 1][0], pp[1][2 * pr - 1][1], pp[1][2 * pr][0], pp[1][2 * pr][1]};
          }
          f32x4& o = hsel == 0 ? o0[UB + u] : o1[UB + u];
          if constexpr (FIRST && F0c.value != 0) mfma_v0(o, hsel == 0 ? vq0[pr] : vq1[pr], pf); else mfma_v(o, hsel == 0 ? vq0[pr] : vq1[pr], pf);
        };
        auto rdy = [](int u, int t) constexpr -> int {
          if (!MASKED) return (t & 1) ? t >> 1 : -1;
          if (u == 0) return t == 0 ? 4 : ((t & 1) ? t >> 1 : -1);
          return t == 0 ? 0 : (t == 7 ? 4 : ((t & 1) == 0 ? t >> 1 : -1));
        };
        auto firstp = [](int u) constexpr -> int { return MASKED ? (u == 0 ? 4 : 0) : 0; };
        sfor<10>([&](auto T) {
          constexpr int t = T.value;
          sfor<2>([&](auto Uc) {
            constexpr int u = Uc.value;
            if constexpr (t < 8) { s[u][t][0] = __builtin_amdgcn_exp2f(s[u][t][0]); s[u][t][1] = __builtin_amdgcn_exp2f(s[u][t][1]); }
            SBAR();
            if constexpr (t >= 2 && rdy(u, t - 2) >= 0) pv(IC<rdy(u, t - 2)>{}, Uc, IC<0>{}, IC<rdy(u, t - 2) == firstp(u)>{});
            else if constexpr (PIPE && t < 8 && 2 * t + u < NKN && t < 5) read_k(D + 1, 2 * t + u);
            SBAR();
            if constexpr (t < 8) { s[u][t][2] = __builtin_amdgcn_exp2f(s[u][t][2]); s[u][t][3] = __builtin_amdgcn_exp2f(s[u][t][3]); }
            if constexpr (t >= 1 && t <= 8) {
              pp[u][t - 1][0] = pk2(s[u][t - 1][0], s[u][t - 1][1]); pp[u][t - 1][1] = pk2(s[u][t - 1][2], s[u][t - 1][3]);
              if constexpr (MASKED && t == 1) { pe[u][0] = pp[u][0][0] & mA0; pe[u][1] = pp[u][0][1] & mA1; pe[u][2] = pp[u][0][0] & ~mA0; pe[u][3] = pp[u][0][1] & ~mA1; }
            }
            SBAR();
            if constexpr (t >= 2 && rdy(u, t - 2) >= 0) {
              pv(IC<rdy(u, t - 2)>{}, Uc, IC<1>{}, IC<rdy(u, t - 2) == firstp(u)>{});
              if constexpr (PIPE && 2 * t + u < NKN && t < 5) read_k(D + 1, 2 * t + u);
            }
            SBAR();
          });
          if constexpr (t < 8) bg(IC<1 + t>{});
        });
        if constexpr (PIPE) make_q();
        SBAR();
        if constexpr (!FIRST) {
          MFMA_DRAIN();
#pragma unroll
          for (int u = 0; u < 2; ++u) {
            char* qrow = qrow_of(D, u);
            const float inv = __builtin_amdgcn_rcpf(bcast_row1(o1[UB + u][0]));
            *(v2u*)(qrow + 8 * gq) = v2u{pk2(o0[UB + u][0] * inv, o0[UB + u][1] * inv), pk2(o0[UB + u][2] * inv, o0[UB + u][3] * inv)};
            *(v2u*)(gq == 0 ? qrow + 32 : lds + LDS_KVB + 8 * TP + 2816 + lane * 2 - (lane & 3) * 2 + 0) = v2u{pk2(o1[UB + u][0] * inv, o1[UB + u][1] * inv), pk2(o1[UB + u][2] * inv, o1[UB + u][3] * inv)};
          }
        }
        SBAR();
      };
      auto nobg = [&](auto) {};
      auto store_o = [&](auto SUBc, auto&& mid) {
        constexpr int SUB = SUBc.value;
        BAR();
        v4u v[2];
        const int mt = (tid >> 4) & 3;
        v[0] = *(const v4u*)(lds + LDS_Q + (SUB * 64 + 16 * mt + r) * 80 + 16 * w);
        v[1] = *(const v4u*)(lds + LDS_Q + (SUB * 64 + 16 * mt + r) * 80 + 64);
        mid(IC<0>{});
        SBAR();
        const unsigned vo = (unsigned)((mt >> 1) * 40960 + (mt & 1) * 1024 + r * 16), ob = LB + L_O + p * 163840 + SUB * 81920;
        { const int c8 = 5 * g + w; stc(v[0], rsW, vo, ob + (c8 >> 2) * 2048 + (c8 & 3) * 256); }
        if (w == 0) { const int c8 = 5 * g + 4; stc(v[1], rsW, vo, ob + (c8 >> 2) * 2048 + (c8 & 3) * 256); }
        asm volatile("" ::: "memory");
      };
      {
        v4u sv[3];
#pragma unroll
        for (int i = 0; i < 3; ++i) sv[i] = *(const v4u*)(lds + LDS_KVB + 8 * TP + (tid + 256 * i) * 16);
        load_kq(IC<0>{});
#pragma unroll
        for (int i = 0; i < 3; ++i) stc(sv[i], rsW, (unsigned)(tid + 256 * i) * 16, LB + L_KV + (p * 16 + g) * 24576 + 12288);
        asm volatile("" ::: "memory");
      }
      dchunk(IC<0>{}, IC<1>{}, [&](auto I) {
        if constexpr (I.value == 2) set_flag<0>(rsW, flag_off(layer, PH_KV, p, 1) + (g * 4 + w) * 4);
        if constexpr (I.value >= 3 && I.value <= 7) ldw<35 + I.value - 3>(rsW, l16, rot5<20 + I.value - 3>(wob, w >> 1));
      });
      dchunk(IC<1>{}, IC<0>{}, [&](auto I) {
        if constexpr (I.value == 0) fv = poll_issue(rsW, kvf_v, kvf_s);
        if constexpr (I.value == 4) {
          poll_check<0>(fv, rsW, kvf_v, kvf_s, p > 0);
          sfor<3>([&](auto K) { dma2<LDS_KVB + 4 * TP + K.value * 4096>(rsW, l16, kb1 + K.value * 4096, lkb); });
        }
        if constexpr (I.value == 5) sfor<3>([&](auto K) { dma2<LDS_KVB + K.value * 4096>(rsW, l16, kb2 + K.value * 4096, lkb); });
      });
      STAMP(5);
      vmcnt<0>();
      BAR();
      STAMP(6);
      load_kq(IC<2>{});
      dchunk(IC<2>{}, IC<1>{}, nobg);
      store_o(IC<0>{}, nobg);
      STAMP(7);
      dchunk(IC<3>{}, IC<0>{}, [&](auto I) {
        if constexpr (I.value == 1) set_flag<0>(rsW, flag_off(layer, PH_O, p, 0) + (g * 4 + w) * 4);
        if constexpr (I.value == 8) fv = poll_issue(rsW, lane * 4, flag_off(layer, PH_O, p, 0));
      });
      unsigned ob = LB + L_O + p * 163840 + rh * 40960 + w * 2048; OPAQUE(ob);
      store_o(IC<1>{}, [&](auto) {
        poll_check<0>(fv, rsW, lane * 4, flag_off(layer, PH_O, p, 0));
#pragma unroll
        for (int i = 0; i < 5; ++i)
#pragma unroll
          for (int m = 0; m < 2; ++m) af[i * 2 + m] = ldc(rsW, l16 + (m ^ (w & 1)) * 1024, ob + i * 8192);
        asm volatile("" ::: "memory");
      });
      STAMP(8);
    }
    auto reduce_epilogue = [&](auto SUBc, f32x4 (&acc)[5][2], unsigned xoff, unsigned soff, bool last, int tid, int w, int p, int g, auto&& mid) {
      constexpr int SUB = SUBc.value;
      const int lane = tid & 63, r = tid & 15, gq = (tid >> 4) & 3, rh = g & 1, c = g >> 1, mo = w & 1, h = w >> 1;
      char* rb = lds + LDS_RED + lane * 16;
#pragma unroll
      for (int n = 0; n < 5; ++n) {
        const int np = (n + 3 * h) % 5;
        *(f32x4*)(rb + (((mo ^ 1) * 5 + np) * 4 + w) * 1024) = acc[n][1];
        if (n >= 2) *(f32x4*)(rb + ((mo * 5 + np) * 4 + w) * 1024) = acc[n][0];
      }
      mid(IC<0>{});
      BAR();
      f32x4 rd[3][3];
#pragma unroll
      for (int n = 0; n < 3; ++n)
#pragma unroll
        for (int q = 1; q < 4; ++q) rd[n][q - 1] = *(const f32x4*)(rb + ((mo * 5 + (n + 3 * h) % 5) * 4 + ((w + q) & 3)) * 1024);
      mid(IC<1>{});
      SBAR();
      float sm = 0.f, sq = 0.f;
#pragma unroll
      for (int n = 0; n < 3; ++n) {
        const f32x4 sv = (acc[n][0] + rd[n][0]) + (rd[n][1] + rd[n][2]);
        const unsigned a0 = pk2(sv[0], sv[1]), a1 = pk2(sv[2], sv[3]);
        const v2u o = {pk2(bf2f(res[SUB][n][0] & 0xffffu) + bf2f(a0 & 0xffffu), bf2f(res[SUB][n][0] >> 16) + bf2f(a0 >> 16)),
                       pk2(bf2f(res[SUB][n][1] & 0xffffu) + bf2f(a1 & 0xffffu), bf2f(res[SUB][n][1] >> 16) + bf2f(a1 >> 16))};
        res[SUB][n] = o;
        if (n < 2 || h == 0) {
          const int np = (n + 3 * h) % 5;
          sm = dot2(o[0], 0x3f803f80u, sm); sq = dot2(o[0], o[0], sq); sm = dot2(o[1], 0x3f803f80u, sm); sq = dot2(o[1], o[1], sq);
          if (!last) {
            const int c8 = 10 * c + 2 * np;
            stc2(o, rsW, (unsigned)((gq >> 1) * 256 + r * 16 + (gq & 1) * 8), xoff + SUB * 81920 + (2 * rh + mo) * 1024 + (c8 >> 2) * 4096 + (c8 & 3) * 256);
          } else {
            *(v2u*)(out + (size_t)(128 * p + 64 * SUB + 32 * rh + 16 * mo + r) * 640 + 80 * c + 16 * np + 4 * gq) = o;
          }
        }
      }
      if (!last) {
        sm = sum32(sum16(sm)); sq = sum32(sum16(sq));
        if (lane < 16) stc2(v2u{__builtin_bit_cast(unsigned, sm), __builtin_bit_cast(unsigned, sq)}, rsW, (unsigned)(r * 128),
                            soff + (64 * SUB + 32 * rh + 16 * mo) * 128 + (c * 2 + h) * 8);
      }
      asm volatile("" ::: "memory");
    };
    {
      FRESH();
      unsigned ob = LB + L_O + p * 163840 + rh * 40960 + w * 2048; OPAQUE(ob);
      unsigned wgb = WG_OFF + (g * 240 + w) * 1024; OPAQUE(wgb);
      unsigned fv;
      STAMP(9);
      f32x4 acc[5][2];
      auto p3_mma = [&](auto OFFc, auto&& mid) {
        constexpr int OFF = OFFc.value;
        sfor<5>([&](auto I) {
          mid(I);
          sfor<2>([&](auto M) { sfor<5>([&](auto N) {
            constexpr int i = I.value, m = M.value, n = N.value;
            if constexpr (i == 0) mfma_w0<40 + n>(acc[n][m], af[OFF + m]); else mfma_w<(i < 4 ? 40 + i * 5 + n : 35 + n)>(acc[n][m], af[OFF + i * 2 + m]);
          }); });
        });
        MFMA_DRAIN();
      };
      p3_mma(IC<0>{}, [&](auto I) { if constexpr (I.value == 2) set_flag<0>(rsW, flag_off(layer, PH_O, p, 1) + (g * 4 + w) * 4); });
      reduce_epilogue(IC<0>{}, acc, LB + L_X2 + p * 163840, LB + L_S2 + p * 16384, false, tid, w, p, g, [&](auto I) {
        if constexpr (I.value == 0) fv = poll_issue(rsW, lane * 4, flag_off(layer, PH_O, p, 1));
        if constexpr (I.value == 1) {
          poll_check<0>(fv, rsW, lane * 4, flag_off(layer, PH_O, p, 1));
#pragma unroll
          for (int i = 0; i < 5; ++i)
#pragma unroll
            for (int m = 0; m < 2; ++m) af[10 + i * 2 + m] = ldc(rsW, l16 + (m ^ (w & 1)) * 1024, ob + 81920 + i * 8192);
        }
      });
      STAMP(10);
      BAR();
      p3_mma(IC<10>{}, [&](auto I) { if constexpr (I.value == 3) set_flag<0>(rsW, flag_off(layer, PH_X2, p, 0) + (g * 4 + w) * 4); });
      reduce_epilogue(IC<1>{}, acc, LB + L_X2 + p * 163840, LB + L_S2 + p * 16384, false, tid, w, p, g, [&](auto I) {
        if constexpr (I.value == 0) sfor<GLATE0 / 2>([&](auto K) { ldw<35 + K.value>(rsW, l16, wgb + (35 + K.value) * 4096); });
        else sfor<GLATE0 / 2>([&](auto K) { ldw<35 + GLATE0 / 2 + K.value>(rsW, l16, wgb + (35 + GLATE0 / 2 + K.value) * 4096); });
      });
      STAMP(11);
      fv = poll_issue(rsW, lane * 4, flag_off(layer, PH_X2, p, 0));
      set_flag<1>(rsW, flag_off(layer, PH_X2, p, 1) + (g * 4 + w) * 4);
      poll_check<0>(fv, rsW, lane * 4, flag_off(layer, PH_X2, p, 0));
      STAMP(12);
      st0 = ldc_asm(rsW, tid * 32, LB + L_S2 + p * 16384); st1 = ldc_asm(rsW, tid * 32 + 16, LB + L_S2 + p * 16384);
      unsigned ab = LB + L_X2 + p * 163840 + w * 1024, lb = LDS_R0 + w * 1024; OPAQUE(ab); OPAQUE(lb);
      sfor<20>([&](auto I) { dma2<I.value * 4096>(rsW, l16, ab + I.value * 4096, lb); });
    }
    {
      FRESH();
      f32x4 c4[3];
#pragma unroll
      for (int j = 0; j < 3; ++j) {
        const v2u cg = __builtin_amdgcn_raw_buffer_load_b64(rsW, gq * 8, CS_OFF + (1920 + 96 * g + 24 * w + 8 * j) * 4, 0);
        const v2u cu = __builtin_amdgcn_raw_buffer_load_b64(rsW, gq * 8, CS_OFF + (3456 + 96 * g + 24 * w + 8 * j) * 4, 0);
        c4[j] = __builtin_bit_cast(f32x4, v4u{cg[0], cg[1], cu[0], cu[1]});
      }
      asm volatile("" ::: "memory");
      unsigned abase = LB + L_X2 + p * 163840 + 81920 + w * 1024, lb1 = LDS_R1 + w * 1024;
      unsigned wgb = WG_OFF + (g * 240 + w) * 1024, wdb = WD_OFF + (c * 240 + w) * 1024;
      unsigned hb = LB + L_H + p * 393216 + rh * 98304 + w * 2048;
      OPAQUE(abase); OPAQUE(lb1); OPAQUE(wgb); OPAQUE(wdb); OPAQUE(hb);
      f32x4 acc[3][4];
      unsigned fv = 1u;
      f32x2 st[4]; v4u sv[3];
      const int mt = (tid >> 4) & 3;
      const unsigned lm0 = l16 + (w & 1) * 1024, lm1 = l16 + ((w & 1) ^ 1) * 1024;
      auto epilogue4 = [&](auto SUBc, auto&& mid) {
        constexpr int SUB = SUBc.value;
        mid(IC<0>{});
        SBAR();
        sfor<3>([&](auto J) {
          constexpr int j = J.value;
          const f32x2 ca = {c4[j][0], c4[j][1]}, cb = {c4[j][2], c4[j][3]};
          sfor<4>([&](auto M) {
            constexpr int m = M.value;
            const f32x2 nm = {-st[m][0], -st[m][0]}, rs2 = {st[m][1], st[m][1]};
            const float rl = st[m][1] * -1.4426950408889634f;
            f32x2 ge = __builtin_elementwise_fma(nm, ca, f32x2{acc[j][m][0], acc[j][m][1]}) * f32x2{rl, rl};
            f32x2 uu = __builtin_elementwise_fma(nm, cb, f32x2{acc[j][m][2], acc[j][m][3]}) * rs2;
            f32x2 dn = f32x2{__builtin_amdgcn_exp2f(ge[0]), __builtin_amdgcn_exp2f(ge[1])} + f32x2{1.f, 1.f};
            f32x2 hv = uu * f32x2{__builtin_amdgcn_rcpf(dn[0]), __builtin_amdgcn_rcpf(dn[1])};
            *(unsigned*)(lds + LDS_H + SUB * 16384 + (m * 16 + r) * HP + (24 * w + 8 * j + 2 * gq) * 2) = pk2(hv[0], hv[1]);
          });
          sfor<4>([&](auto M) { mid(IC<1 + j * 4 + M.value>{}); });
          SBAR();
        });
      };
      struct G8 { static constexpr const Sched& get() { return SS8; } };
      ring_gemm(IC<3>{}, IC<0>{}, IC<0>{}, SS8, G8{}, acc, l16, [&](auto OP, auto KS, auto M) {
        constexpr int op = OP.value;
        if constexpr (op >= 2000 && op < 3000) ldw<op - 2000>(rsW, l16, wgb + (op - 2000) * 4096);
        else if constexpr (op >= 1000 && op < 2000) dma2<(op - 1000) * 4096>(rsW, l16, abase + (op - 1000) * 4096, lb1);
        else if constexpr (op == 3005) asm volatile("" : "+v"(c4[0]), "+v"(c4[1]), "+v"(c4[2]));
        else if constexpr (op == 3001) fv = poll_issue(rsW, lane * 4, flag_off(layer, PH_X2, p, 1));
        else if constexpr (op == 3002) poll_check<ops_after_code(SS8, 3001, KS.value, M.value)>(fv, rsW, lane * 4, flag_off(layer, PH_X2, p, 1));
        else if constexpr (op == 3003) { st0 = ldc_asm(rsW, tid * 32, LB + L_S2 + p * 16384 + 8192); st1 = ldc_asm(rsW, tid * 32 + 16, LB + L_S2 + p * 16384 + 8192); }
        else if constexpr (op >= 3010 && op < 3016) stats_stage(IC<op - 3010>{}, 0, tid);
        else if constexpr (op == 3030) {
#pragma unroll
          for (int m = 0; m < 4; ++m) st[m] = *(const f32x2*)(lds + LDS_ST + (m * 16 + r) * 8);
        }
      });
      STAMP(13);
      epilogue4(IC<0>{}, [&](auto I) { if constexpr (I.value == 0) dma2<LDS_R1X - LDS_R1>(rsW, l16, abase + 19 * 4096, lb1); });
      STAMP(14);
      struct G9 { static constexpr const Sched& get() { return SS9; } };
      ring_gemm(IC<3>{}, IC<1>{}, IC<0>{}, SS9, G9{}, acc, l16, [&](auto OP, auto KS, auto M) {
        constexpr int op = OP.value;
        if constexpr (op >= 2000 && op < 3000) ldw<op - 2000>(rsW, l16, rot5<op - 2000>(wdb, w >> 1));
        else if constexpr (op == 3000) set_flag<ops_after_code(SS9, 3023, KS.value, M.value)>(rsW, flag_off(layer, PH_H, p, 0) + (g * 4 + w) * 4);
        else if constexpr (op == 3001) fv = poll_issue(rsW, lane * 4, flag_off(layer, PH_H, p, 0));
        else if constexpr (op == 3002) poll_check<ops_after_code(SS9, 3001, KS.value, M.value)>(fv, rsW, lane * 4, flag_off(layer, PH_H, p, 0));
        else if constexpr (op >= 3010 && op < 3016) stats_stage(IC<op - 3010>{}, 1, tid);
        else if constexpr (op == 3020) {
#pragma unroll
          for (int i = 0; i < 3; ++i) sv[i] = *(const v4u*)(lds + LDS_H + (16 * mt + r) * HP + 16 * (w + 4 * i));
        } else if constexpr (op > 3020 && op <= 3023) {
          stc(sv[op - 3021], rsW, (unsigned)((mt >> 1) * 98304 + (mt & 1) * 1024 + r * 16), LB + L_H + p * 393216 + (3 * g + op - 3021) * 2048 + w * 256);
          asm volatile("" ::: "memory");
        } else if constexpr (op == 3030) {
#pragma unroll
          for (int m = 0; m < 4; ++m) st[m] = *(const f32x2*)(lds + LDS_ST + (64 + m * 16 + r) * 8);
        }
        else if constexpr (op >= 5000) { constexpr int i = op - 5000; af[i] = ldc_asm(rsW, (i & 1) ? lm1 : lm0, hb + (i >> 1) * 8192); }
      });
      STAMP(15);
      epilogue4(IC<1>{}, [&](auto I) {
        constexpr int i = I.value;
        if constexpr (i >= 1 && i <= 8) { af[6 + 2 * i] = ldc_asm(rsW, lm0, hb + (3 + i) * 8192); af[7 + 2 * i] = ldc_asm(rsW, lm1, hb + (3 + i) * 8192); }
        else if constexpr (i >= 9) sfor<3>([&](auto K) { constexpr int f = NBD9 + (i - 9) * 3 + K.value; ldw<f>(rsW, l16, rot5<f>(wdb, w >> 1)); });
      });
      sfor<60 - NBD9 - 12>([&](auto K) { constexpr int f = NBD9 + 12 + K.value; ldw<f>(rsW, l16, rot5<f>(wdb, w >> 1)); });
      STAMP(16);
    }
    {
      FRESH();
      unsigned hb = LB + L_H + p * 393216 + 196608 + rh * 98304 + w * 2048; OPAQUE(hb);
      unsigned wqb = WQ_OFF + (g * 160 + w) * 1024; OPAQUE(wqb);
      f32x4 acc[5][2];
      unsigned fv = 1u;
      asm volatile("s_waitcnt vmcnt(14)" : "+v"(af[0]), "+v"(af[1]), "+v"(af[2]), "+v"(af[3]), "+v"(af[4]), "+v"(af[5]), "+v"(af[6]), "+v"(af[7]), "+v"(af[8]), "+v"(af[9]), "+v"(af[10]), "+v"(af[11]),
                   "+v"(af[12]), "+v"(af[13]), "+v"(af[14]), "+v"(af[15]), "+v"(af[16]), "+v"(af[17]), "+v"(af[18]), "+v"(af[19]), "+v"(af[20]), "+v"(af[21]), "+v"(af[22]), "+v"(af[23]) :: "memory");
      BAR();
      v4u sv[3];
      const int mt = (tid >> 4) & 3;
      sfor<12>([&](auto I) {
        constexpr int i = I.value;
        if constexpr (i == 8) set_flag<0>(rsW, flag_off(layer, PH_H, p, 1) + (g * 4 + w) * 4);
        sfor<2>([&](auto M) { sfor<5>([&](auto N) {
          constexpr int m = M.value, n = N.value;
          if constexpr (i == 0) mfma_w0<n>(acc[n][m], af[m]); else mfma_w<i * 5 + n>(acc[n][m], af[i * 2 + m]);
          if constexpr (i == 0 && m == 0 && n < 3) sv[n] = *(const v4u*)(lds + LDS_H + 16384 + (16 * mt + r) * HP + 16 * (w + 4 * n));
          if constexpr (i == 1 && m == 0 && n < 3) {
            stc(sv[n], rsW, (unsigned)((mt >> 1) * 98304 + (mt & 1) * 1024 + r * 16), LB + L_H + p * 393216 + 196608 + (3 * g + n) * 2048 + w * 256);
            asm volatile("" ::: "memory");
          }
          SBAR();
        }); });
      });
      MFMA_DRAIN();
      STAMP(17);
      reduce_epilogue(IC<0>{}, acc, LB + L_X3 + p * 163840, LB + L_S3 + p * 16384, layer == 1, tid, w, p, g, [&](auto I) {
        if constexpr (I.value == 1) fv = poll_issue(rsW, lane * 4, flag_off(layer, PH_H, p, 1));
      });
      STAMP(18);
      poll_check<0>(fv, rsW, lane * 4, flag_off(layer, PH_H, p, 1));
      STAMP(19);
#pragma unroll
      for (int i = 0; i < 12; ++i)
#pragma unroll
        for (int m = 0; m < 2; ++m) af[i * 2 + m] = ldc(rsW, l16 + (m ^ (w & 1)) * 1024, hb + i * 8192);
      if (layer == 0) set_flag<24>(rsW, flag_off(layer, PH_X3, p, 0) + (g * 4 + w) * 4);
      BAR();
      SBAR();
      sfor<12>([&](auto I) {
        constexpr int i = I.value;
        sfor<2>([&](auto M) {
          constexpr int m = M.value;
          sfor<5>([&](auto N) { constexpr int n = N.value; if constexpr (i == 0) mfma_w0<n>(acc[n][m], af[m]); else mfma_w<i * 5 + n>(acc[n][m], af[i * 2 + m]); });
          if constexpr (i >= 4) { constexpr int k = (i - 4) * 2 + m; ldw<k>(rsW, l16, wqb + k * 4096); }
          SBAR();
        });
      });
      MFMA_DRAIN();
      STAMP(20);
      reduce_epilogue(IC<1>{}, acc, LB + L_X3 + p * 163840, LB + L_S3 + p * 16384, layer == 1, tid, w, p, g, [&](auto I) {
        if (layer == 0) sfor<12>([&](auto K) { constexpr int k = 16 + I.value * 12 + K.value; ldw<k>(rsW, l16, wqb + k * 4096); });
      });
      STAMP(21);
      if (layer == 0) {
        fv = poll_issue(rsW, lane * 4, flag_off(0, PH_X3, p, 0));
        set_flag<1>(rsW, flag_off(0, PH_X3, p, 1) + (g * 4 + w) * 4);
        poll_check<0>(fv, rsW, lane * 4, flag_off(0, PH_X3, p, 0));
        st0 = ldc_asm(rsW, tid * 32, LB + L_S3 + p * 16384); st1 = ldc_asm(rsW, tid * 32 + 16, LB + L_S3 + p * 16384);
        unsigned ab = LB + L_X3 + p * 163840 + w * 1024, lb = LDS_R0 + w * 1024; OPAQUE(ab); OPAQUE(lb);
        sfor<20>([&](auto I) { dma2<I.value * 4096>(rsW, l16, ab + I.value * 4096, lb); });
      }
      STAMP(22);
    }
  }
}

extern "C" void kernel_launch(void* const* d_in, const int* in_sizes, int n_in, void* d_out, int out_size, void* d_ws,
                              size_t ws_size, hipStream_t stream) {
  const u16* x = (const u16*)d_in[0];
  (void)hipFuncSetAttribute((const void*)k_main, hipFuncAttributeMaxDynamicSharedMemorySize, LDS_BYTES);
  k_prep<<<PREP_GRID, 256, 0, stream>>>(x, (const u16*)d_in[1], (const u16*)d_in[2], (const u16*)d_in[4], (const u16*)d_in[5],
                                        (const u16*)d_in[6], (char*)d_ws);
  k_main<<<256, 256, LDS_BYTES, stream>>>(x, (u16*)d_out, (char*)d_ws);
}
```

```cpp
#include <hip/hip_runtime.h>
#include <hip/hip_bf16.h>
#include <stdint.h>
#include <utility>

typedef unsigned short u16;
typedef unsigned v4u __attribute__((ext_vector_type(4)));
typedef unsigned v2u __attribute__((ext_vector_type(2)));
typedef float f32x4 __attribute__((ext_vector_type(4)));
typedef float f32x2 __attribute__((ext_vector_type(2)));
typedef short s16x4 __attribute__((ext_vector_type(4)));
typedef __bf16 bf16x2 __attribute__((ext_vector_type(2)));
typedef __bf16 bf16x8 __attribute__((ext_vector_type(8)));
#define GAS __attribute__((address_space(1)))
#define LAS __attribute__((address_space(3)))
#define DI __device__ __forceinline__
#define RLX_AGENT __ATOMIC_RELAXED, __HIP_MEMORY_SCOPE_AGENT
typedef GAS unsigned long long gu64;

template <int I> struct IC { static constexpr int value = I; };
template <class F, int... I> DI void sfor_(F&& f, std::integer_sequence<int, I...>) { (f(IC<I>{}), ...); }
template <int N, class F> DI void sfor(F&& f) { sfor_(f, std::make_integer_sequence<int, N>{}); }

constexpr int S_ = 2048, D_ = 640, F_ = 1536;
constexpr unsigned FLAG_OFF = 0, FLAG_BYTES = 262144;
constexpr unsigned RF_OFF = 163840;
constexpr unsigned EPOCH_OFF = 172032;
constexpr unsigned WQ_OFF = FLAG_BYTES;
constexpr unsigned WO_OFF = WQ_OFF + 16 * 160 * 1024;
constexpr unsigned WG_OFF = WO_OFF + 8 * 100 * 1024;
constexpr unsigned WD_OFF = WG_OFF + 16 * 240 * 1024;
constexpr unsigned CS_OFF = WD_OFF + 8 * 240 * 1024;
constexpr unsigned NULL_OFF = CS_OFF + 20480;
constexpr unsigned STX_OFF = CS_OFF + 32768;
constexpr unsigned ST_BYTES = 2048 * 128;
constexpr unsigned X0_OFF = STX_OFF + ST_BYTES;
constexpr unsigned ACT_OFF = X0_OFF + 16 * 160 * 1024;
constexpr unsigned KV_BYTES = 16 * 16 * 24 * 1024;
constexpr unsigned XF_BYTES = 16 * 160 * 1024;
constexpr unsigned HF_BYTES = 16 * 384 * 1024;
constexpr unsigned L_KV = 0, L_O = L_KV + KV_BYTES, L_X2 = L_O + XF_BYTES, L_H = L_X2 + XF_BYTES, L_X3 = L_H + HF_BYTES,
                   L_S2 = L_X3 + XF_BYTES, L_S3 = L_S2 + ST_BYTES, LAYER_BYTES = L_S3 + ST_BYTES;
constexpr unsigned WS_USED = ACT_OFF + 2 * LAYER_BYTES;
constexpr unsigned STAMP_OFF = WS_USED;
enum { PH_KV = 0, PH_O = 1, PH_X2 = 2, PH_H = 3, PH_X3 = 4 };

constexpr int TP = 3072;
constexpr int LDS_KVA = 0, LDS_KVB = 12 * TP;
constexpr int LDS_Q = 24 * TP;
constexpr int LDS_R0 = 0;
constexpr int LDS_R1 = 83968;
constexpr int LDS_R1X = LDS_KVB + 8 * TP;
constexpr int LDS_RED = LDS_R1;
constexpr int RP = 336;
constexpr int LDS_H = 0;
constexpr int HP = 208;
constexpr int LDS_ST = LDS_R1 + 19 * 4096;
constexpr int LDS_BYTES = LDS_ST + 1024;

DI float bf2f(unsigned b16) { return __builtin_bit_cast(float, b16 << 16); }
DI unsigned pk2(float a, float b) {
  bf16x2 r; r[0] = (__bf16)a; r[1] = (__bf16)b; return __builtin_bit_cast(unsigned, r);
}
DI float rbf(float a) { return bf2f(pk2(a, 0.f) & 0xffffu); }
template <int SL> DI void mfma_w(f32x4& c, const v4u& xv) { asm volatile("v_mfma_f32_16x16x32_bf16 %0, a[%c2:%c3], %1, %0" : "+v"(c) : "v"(xv), "i"(4 * SL), "i"(4 * SL + 3)); }
template <int SL> DI void mfma_w0(f32x4& c, const v4u& xv) { asm volatile("v_mfma_f32_16x16x32_bf16 %0, a[%c2:%c3], %1, 0" : "=&v"(c) : "v"(xv), "i"(4 * SL), "i"(4 * SL + 3)); }
template <int SL> DI void ldw(__amdgpu_buffer_rsrc_t rs, unsigned voff, unsigned soff) {
  asm volatile("buffer_load_dwordx4 a[%c3:%c4], %0, %1, %2 offen sc1" :: "v"(voff), "s"(rs), "s"(soff), "i"(4 * SL), "i"(4 * SL + 3) : "memory");
}
DI void mfma_v(f32x4& c, const v4u& a, const v4u& b) { asm volatile("s_nop 1\n\tv_mfma_f32_16x16x32_bf16 %0, %1, %2, %0" : "+v"(c) : "v"(a), "v"(b)); }
DI void mfma_v0(f32x4& c, const v4u& a, const v4u& b) { asm volatile("s_nop 1\n\tv_mfma_f32_16x16x32_bf16 %0, %1, %2, 0" : "=&v"(c) : "v"(a), "v"(b)); }
DI void mfma_n(f32x4& c, const v4u& a, const v4u& b) { asm volatile("v_mfma_f32_16x16x32_bf16 %0, %1, %2, %0" : "+v"(c) : "v"(a), "v"(b)); }
DI void mfma_n0(f32x4& c, const v4u& a, const v4u& b) { asm volatile("v_mfma_f32_16x16x32_bf16 %0, %1, %2, 0" : "=&v"(c) : "v"(a), "v"(b)); }
#define MFMA_DRAIN() do { __builtin_amdgcn_sched_barrier(0); asm volatile("s_nop 7\n\ts_nop 7\n\ts_nop 3" ::: "memory"); __builtin_amdgcn_sched_barrier(0); } while (0)
DI float dot2(unsigned a, unsigned b, float c) {
  return __builtin_amdgcn_fdot2_f32_bf16(__builtin_bit_cast(bf16x2, a), __builtin_bit_cast(bf16x2, b), c, false);
}
DI float max3(float a, float b, float c) { float d; asm("v_max3_f32 %0, %1, %2, %3" : "=v"(d) : "v"(a), "v"(b), "v"(c)); return d; }
DI v4u ldb(__amdgpu_buffer_rsrc_t rs, unsigned voff, unsigned soff) { return __builtin_amdgcn_raw_buffer_load_b128(rs, voff, soff, 0); }
DI v4u ldc(__amdgpu_buffer_rsrc_t rs, unsigned voff, unsigned soff) { return __builtin_amdgcn_raw_buffer_load_b128(rs, voff, soff, 16); }
DI void stc(v4u v, __amdgpu_buffer_rsrc_t rs, unsigned voff, unsigned soff) { __builtin_amdgcn_raw_buffer_store_b128(v, rs, voff, soff, 16); }
DI void stc2(v2u v, __amdgpu_buffer_rsrc_t rs, unsigned voff, unsigned soff) { __builtin_amdgcn_raw_buffer_store_b64(v, rs, voff, soff, 16); }
DI void dma(__amdgpu_buffer_rsrc_t rs, unsigned voff, unsigned soff, unsigned lds) {
  unsigned keep;
  asm volatile("s_mov_b32 %0, m0\n\ts_mov_b32 m0, %4\n\ts_nop 0\n\tbuffer_load_dwordx4 %1, %2, %3 offen sc1 lds\n\ts_mov_b32 m0, %0"
               : "=&s"(keep) : "v"(voff), "s"(rs), "s"(soff), "s"(lds) : "memory");
}
template <int LOFF> DI void dma2(__amdgpu_buffer_rsrc_t rs, unsigned voff, unsigned soff, unsigned lbase) {
  asm volatile("s_add_u32 m0, %3, %4\n\ts_nop 0\n\tbuffer_load_dwordx4 %0, %1, %2 offen sc1 lds" :: "v"(voff), "s"(rs), "s"(soff), "s"(lbase), "i"(LOFF) : "memory", "scc");
}
DI float dpp_xor1(float v) { return __builtin_bit_cast(float, __builtin_amdgcn_update_dpp(0, __builtin_bit_cast(int, v), 0xB1, 0xf, 0xf, true)); }
DI float dpp_xor2(float v) { return __builtin_bit_cast(float, __builtin_amdgcn_update_dpp(0, __builtin_bit_cast(int, v), 0x4E, 0xf, 0xf, true)); }
#define OPAQUE(v) asm volatile("" : "+s"(v))
#define FRESH() int tid = tid0, w = w0, p = p0, g = g0; asm volatile("" : "+v"(tid)); asm volatile("" : "+s"(w), "+s"(p), "+s"(g)); const int lane = tid & 63, r = tid & 15, gq = (tid >> 4) & 3, rh = g & 1, c = g >> 1; const unsigned l16 = lane * 16; (void)r; (void)gq; (void)l16; (void)rh; (void)c
template <int N> DI void vmcnt() { asm volatile("s_waitcnt vmcnt(%0)" :: "i"(N > 63 ? 63 : N) : "memory"); }
#define BAR() asm volatile("s_waitcnt lgkmcnt(0)\n\ts_barrier" ::: "memory")
#define SBAR() __builtin_amdgcn_sched_barrier(0)
DI float sum16(float v) { auto t = __builtin_amdgcn_permlane16_swap(__builtin_bit_cast(unsigned, v), __builtin_bit_cast(unsigned, v), false, false);
  return __builtin_bit_cast(float, (unsigned)t[0]) + __builtin_bit_cast(float, (unsigned)t[1]); }
DI float sum32(float v) { auto t = __builtin_amdgcn_permlane32_swap(__builtin_bit_cast(unsigned, v), __builtin_bit_cast(unsigned, v), false, false);
  return __builtin_bit_cast(float, (unsigned)t[0]) + __builtin_bit_cast(float, (unsigned)t[1]); }
DI float max1632(float v) {
  auto t = __builtin_amdgcn_permlane16_swap(__builtin_bit_cast(unsigned, v), __builtin_bit_cast(unsigned, v), false, false);
  float a = __builtin_bit_cast(float, (unsigned)t[0]), b = __builtin_bit_cast(float, (unsigned)t[1]);
  float m = max3(a, b, b);
  auto u = __builtin_amdgcn_permlane32_swap(__builtin_bit_cast(unsigned, m), __builtin_bit_cast(unsigned, m), false, false);
  float c = __builtin_bit_cast(float, (unsigned)u[0]), d = __builtin_bit_cast(float, (unsigned)u[1]);
  return max3(c, d, d);
}
DI float bcast_row1(float v) {
  auto t = __builtin_amdgcn_permlane16_swap(__builtin_bit_cast(unsigned, v), __builtin_bit_cast(unsigned, v), false, false);
  auto u = __builtin_amdgcn_permlane32_swap((unsigned)t[1], (unsigned)t[1], false, false);
  return __builtin_bit_cast(float, (unsigned)u[0]);
}

#define STAMP(i)

DI int qkv_src_col(int g, int pc) {
  if (pc < 40) return 40 * g + pc;
  if (pc < 60) return 640 + 40 * g + (pc - 40);
  if (pc < 80) return 1280 + 40 * g + (pc - 60);
  if (pc < 100) return 640 + 40 * g + 20 + (pc - 80);
  if (pc < 120) return 1280 + 40 * g + 20 + (pc - 100);
  return -1;
}
struct Wts { const u16 *x, *wqkv, *wo, *wg, *wu, *wd; };
DI void prep_load(int s, const Wts& W, v4u (&v)[8], int tid, int w) {
  const int lane = tid & 63;
  const u16* src = W.x; unsigned off = 0, step = 0;
  if (s < 80) {
    const int ch = lane < 60 ? lane : 0, sg = ch >= 40 ? 2 : (ch >= 20 ? 1 : 0);
    src = W.wqkv; off = (32 * (s >> 2) + w) * 1920 + sg * 640 + 160 * (s & 3) + (ch - 20 * sg) * 8; step = 4 * 1920;
  } else if (s < 208) {
    off = ((s - 80) * 16 + w * 4 + (lane >> 4)) * 640 + (lane & 15) * 8; step = 128;
  } else if (s < 368) {
    const int e0 = s - 208, ch = lane < 48 ? lane : 0;
    src = ch < 24 ? W.wg : W.wu; off = (32 * (e0 >> 3) + w) * 1536 + 192 * (e0 & 7) + (ch < 24 ? ch : ch - 24) * 8; step = 4 * 1536;
  } else if (s < 504) {
    const bool isd = s >= 408; const int e0 = isd ? s - 408 : s - 368, ch = lane < 40 ? lane : 0;
    src = isd ? W.wd : W.wo; off = (32 * (e0 >> 1) + w) * 640 + 320 * (e0 & 1) + ch * 8; step = 4 * 640;
  }
  const bool st = s >= 80 && s < 208;
#pragma unroll
  for (int i = 0; i < 8; ++i) v[i] = *(const GAS v4u*)(src + off + ((st && i >= 5) ? 0 : i) * step);
}
DI void prep_store(int s, const v4u (&v)[8], __amdgpu_buffer_rsrc_t rsW, char* sm, int tid, int w) {
  const int lane = tid & 63, gq = lane >> 4, q4 = (lane & 15) >> 2, p4 = lane & 3;
  auto emit = [&](unsigned ldsoff, int pitch, unsigned dst) {
    const char* a = sm + (8 * gq + q4) * pitch + ldsoff;
    v2u lo = __builtin_bit_cast(v2u, __builtin_amdgcn_ds_read_tr16_b64_v4i16((LAS s16x4*)a));
    v2u hi = __builtin_bit_cast(v2u, __builtin_amdgcn_ds_read_tr16_b64_v4i16((LAS s16x4*)(a + 4 * pitch)));
    stc(v4u{lo[0], lo[1], hi[0], hi[1]}, rsW, lane * 16, dst);
  };
  if (s < 80) {
    const int ks = s >> 2, g0 = (s & 3) * 4;
    constexpr int P = 976;
    if (lane <= 60) {
#pragma unroll
      for (int i = 0; i < 8; ++i) *(v4u*)(sm + (w + 4 * i) * P + lane * 16) = lane < 60 ? v[i] : v4u{0, 0, 0, 0};
    }
    BAR();
#pragma unroll
    for (int i = 0; i < 8; ++i) {
      const int pc = 16 * i + 4 * p4;
      const unsigned off = pc < 40 ? 80 * w + pc * 2 : (pc < 60 ? 320 + 80 * w + (pc - 40) * 2 : (pc < 80 ? 640 + 80 * w + (pc - 60) * 2 :
                           (pc < 100 ? 320 + 80 * w + 40 + (pc - 80) * 2 : (pc < 120 ? 640 + 80 * w + 40 + (pc - 100) * 2 : 960))));
      emit(off, P, WQ_OFF + (((g0 + w) * 20 + ks) * 8 + (i & 1) * 4 + (i >> 1)) * 1024);
    }
  } else if (s < 208) {
    const int row = (s - 80) * 16 + w * 4 + gq, r = lane & 15;
    float sm1 = 0.f, sq = 0.f;
#pragma unroll
    for (int i = 0; i < 5; ++i)
#pragma unroll
      for (int e = 0; e < 4; ++e) { sm1 = dot2(v[i][e], 0x3f803f80u, sm1); sq = dot2(v[i][e], v[i][e], sq); }
#pragma unroll
    for (int o = 8; o >= 1; o >>= 1) { sm1 += __shfl_xor(sm1, o); sq += __shfl_xor(sq, o); }
    stc2(r == 0 ? v2u{__builtin_bit_cast(unsigned, sm1), __builtin_bit_cast(unsigned, sq)} : v2u{0u, 0u}, rsW, STX_OFF + (row * 16 + r) * 8, 0);
    const int mt = s - 80;
#pragma unroll
    for (int i = 0; i < 5; ++i)
      stc(v[i], rsW, (unsigned)((r >> 2) * 4096 + (r & 3) * 256 + (w * 4 + gq) * 16), X0_OFF + (mt >> 2) * 81920 + (mt & 3) * 1024 + i * 16384);
  } else if (s < 368) {
    const int e0 = s - 208, ks = e0 >> 3, g0 = (e0 & 7) * 2;
    constexpr int P = 776;
    if (lane < 48) {
      char* d = sm + w * P + (lane < 24 ? lane * 32 : (lane - 24) * 32 + 4);
#pragma unroll
      for (int i = 0; i < 8; ++i)
#pragma unroll
        for (int e = 0; e < 4; ++e) *(unsigned*)(d + 4 * i * P + 8 * e) = v[i][e];
    }
    BAR();
#pragma unroll
    for (int i = 0; i < 6; ++i) {
      const int gl = w >> 1, wv = (w & 1) * 2 + i / 3, j = i % 3;
      emit((w * 6 + i) * 32 + p4 * 8, P, WG_OFF + (((g0 + gl) * 20 + ks) * 12 + j * 4 + wv) * 1024);
    }
  } else if (s < 504) {
    const bool isd = s >= 408; const int e0 = isd ? s - 408 : s - 368;
    const int ks = e0 >> 1, c = 4 * (e0 & 1) + w;
    constexpr int P = 648;
    if (lane < 40) {
#pragma unroll
      for (int i = 0; i < 8; ++i) {
        *(v2u*)(sm + (w + 4 * i) * P + lane * 16) = v2u{v[i][0], v[i][1]};
        *(v2u*)(sm + (w + 4 * i) * P + lane * 16 + 8) = v2u{v[i][2], v[i][3]};
      }
    }
    BAR();
    const unsigned dst = (isd ? WD_OFF + c * 240 * 1024 : WO_OFF + c * 100 * 1024) + ((ks >> 2) * 20 + (ks & 3)) * 1024;
#pragma unroll
    for (int i = 0; i < 5; ++i) emit((w * 80 + i * 16 + p4 * 4) * 2, P, dst + i * 4096);
  } else if (s == 504) {
#pragma unroll
    for (int i = 0; i < 3; ++i) { const int ch = i * 256 + tid, o = (ch * 16) % 3072; stc(v4u{o == 2560 ? 0x3f80u : (o == 2576 ? 0xF14Au : 0u), 0, 0, 0}, rsW, NULL_OFF + ch * 16, 0); }
  }
}

struct Sched { int op[20][4][2]; };
constexpr void put(Sched& s, int ks, int m, int code) { if (s.op[ks][m][0] == 0) s.op[ks][m][0] = code; else s.op[ks][m][1] = code; }
constexpr int weight_of(int c) { return c == 0 ? 0 : (c >= 4000 ? 0 : (c == 3003 ? 2 : (c == 3006 ? 4 : (((c >= 1000 && c < 3000) || c == 3001) ? 1 : 0)))); }
constexpr int ops_before(const Sched& s, int ks_end, int m_end = 0) {
  int n = 0;
  for (int ks = 0; ks < 20; ++ks) for (int m = 0; m < 4; ++m) { if (ks > ks_end || (ks == ks_end && m >= m_end)) return n; n += weight_of(s.op[ks][m][0]) + weight_of(s.op[ks][m][1]); }
  return n;
}
constexpr int ops_after_code(const Sched& s, int code, int ks_end, int m_end = 0) {
  int n = 0; bool seen = false;
  for (int ks = 0; ks < 20; ++ks) for (int m = 0; m < 4; ++m) {
    if (ks > ks_end || (ks == ks_end && m >= m_end)) return n;
    for (int e = 0; e < 2; ++e) { if (seen) n += weight_of(s.op[ks][m][e]); if (s.op[ks][m][e] == code) seen = true; }
  }
  return n;
}
constexpr void put_stats(Sched& s, int ks0) { for (int i = 0; i < 6; ++i) put(s, ks0 + i / 2, (i & 1) * 2, 3010 + i); }
constexpr Sched mk_s1() {
  Sched s{};
  put(s, 2, 1, 3006); put(s, 5, 3, 3007);
  for (int i = 0; i < 20; ++i) put(s, 6 + i / 2, (i & 1) * 2 + 1, 2000 + i);
  put_stats(s, 6); put(s, 12, 0, 3001); put(s, 16, 0, 3002); put(s, 16, 1, 3003);
  for (int i = 0; i < 7; ++i) put(s, 16 + (i + 2) / 4 + (i >= 6), i < 6 ? (i + 2) % 4 : 3, 1000 + i);
  put(s, 18, 0, 3030);
  return s;
}
constexpr Sched mk_s2() {
  Sched s{};
  put(s, 0, 0, 3020); put(s, 1, 0, 3021); put(s, 1, 2, 3022); put(s, 2, 0, 3023);
  for (int i = 0; i < 35; ++i) put(s, 1 + i / 2, (i & 1) * 2 + 1, 2000 + i);
  put_stats(s, 5); put(s, 9, 0, 3000); put(s, 15, 0, 3001); put(s, 18, 2, 3002);
  put(s, 18, 2, 4000); put(s, 18, 3, 4001); put(s, 19, 0, 4002); put(s, 19, 1, 4003); put(s, 19, 2, 4004); put(s, 19, 3, 4005);
  put(s, 18, 0, 3030);
  return s;
}
constexpr int GLATE0 = 12;
constexpr Sched mk_s8() {
  Sched s{};
  for (int i = GLATE0; i < 25; ++i) put(s, 1 + (i - GLATE0) / 2, ((i - GLATE0) & 1) * 2 + 1, 2000 + 35 + i);
  put_stats(s, 5); put(s, 8, 0, 3001); put(s, 11, 0, 3002); put(s, 11, 1, 3003);
  for (int i = 0; i < 19; ++i) put(s, 11 + (i + 2) / 3, 1 + (i + 2) % 3, 1000 + i);
  put(s, 18, 0, 3030);
  return s;
}
constexpr int NBD9 = 46;
constexpr Sched mk_s9() {
  Sched s{};
  put(s, 0, 0, 3020); put(s, 0, 2, 3021); put(s, 0, 3, 3022); put(s, 1, 0, 3023);
  int f = 0;
  for (int ks = 1; ks < 20; ++ks) for (int m = 1; m < 4; ++m) { if (ks >= 12 && m == 2) continue; if (f < NBD9 && f < 3 * ks) put(s, ks, m, 2000 + f++); }
  put_stats(s, 3); put(s, 6, 0, 3000); put(s, 13, 0, 3001); put(s, 16, 0, 3002);
  for (int i = 0; i < 8; ++i) put(s, 16 + i / 2, 2, 5000 + i);
  put(s, 18, 0, 3030);
  return s;
}
constexpr Sched SS1 = mk_s1(), SS2 = mk_s2(), SS8 = mk_s8(), SS9 = mk_s9();
constexpr int count_w(const Sched& s) { int n = 0; for (int ks = 0; ks < 20; ++ks) for (int m = 0; m < 4; ++m) for (int e = 0; e < 2; ++e) if (s.op[ks][m][e] >= 2000 && s.op[ks][m][e] < 3000) ++n; return n; }
static_assert(count_w(SS9) == NBD9, "down fragments in P4(b)");

DI v2u poll_issue(__amdgpu_buffer_rsrc_t rs, unsigned voff, unsigned soff) {
  v2u v; asm volatile("buffer_load_dwordx2 %0, %1, %2, %3 offen sc1" : "=&v"(v) : "v"(voff), "s"(rs), "s"(soff) : "memory"); return v;
}
template <int N> DI void poll_check(v2u v, v2u tag, __amdgpu_buffer_rsrc_t rs, unsigned voff, unsigned soff, bool need = true) {
  asm volatile("s_waitcnt vmcnt(%1)" : "+v"(v) : "i"(N > 63 ? 63 : N) : "memory");
  if (need)
#pragma clang loop unroll(disable)
    for (int spins = 0; spins < (1 << 21) && !__all(v[0] == tag[0] && v[1] == tag[1]); ++spins)
      asm volatile("s_sleep 2\n\tbuffer_load_dwordx2 %0, %1, %2, %3 offen sc1\n\ts_waitcnt vmcnt(0)" : "=&v"(v) : "v"(voff), "s"(rs), "s"(soff) : "memory");
  asm volatile("" ::: "memory");
}
template <int N> DI void set_flag(v2u tag, __amdgpu_buffer_rsrc_t rs, unsigned soff) {
  vmcnt<N>();
  __builtin_amdgcn_raw_buffer_store_b64(tag, rs, 0, soff, 16);
  asm volatile("" ::: "memory");
}
DI v4u ldc_asm(__amdgpu_buffer_rsrc_t rs, unsigned voff, unsigned soff) {
  v4u v; asm volatile("buffer_load_dwordx4 %0, %1, %2, %3 offen sc1" : "=&v"(v) : "v"(voff), "s"(rs), "s"(soff) : "memory"); return v;
}
template <int F> DI unsigned rot5(unsigned base, int h) { constexpr int i = F / 5, n = F % 5; return base + (unsigned)(i * 5 + n) * 4096u + (h ? (unsigned)(((n + 3) % 5) - n) * 4096u : 0u); }
DI unsigned flag_off(int layer, int ph, int p, int sub) { return FLAG_OFF + (unsigned)(((layer * 5 + ph) * 16 + p) * 2 + sub) * 512; }

__global__ void __launch_bounds__(256, 1) k_main(Wts W, u16* __restrict__ out, char* __restrict__ ws) {
  const u16* x = W.x;
  extern __shared__ __attribute__((aligned(16))) char lds[];
  const int tid0 = threadIdx.x;
  const int w0 = __builtin_amdgcn_readfirstlane(tid0 >> 6);
  const int b = blockIdx.x, x8 = b & 7, rr = b >> 3;
  const int p0 = (x8 >> 1) * 4 + (rr >> 3), g0 = (x8 & 1) * 8 + (rr & 7);
  const __amdgpu_buffer_rsrc_t rsW = __builtin_amdgcn_make_buffer_rsrc((void*)ws, 0, 0x7fffffff, 0x00020000);
  const __amdgpu_buffer_rsrc_t rsX = __builtin_amdgcn_make_buffer_rsrc((void*)x, 0, S_ * D_ * 2, 0x00020000);

  asm volatile("" ::: "a0", "a1", "a2", "a3", "a4", "a5", "a6", "a7", "a8", "a9", "a10", "a11", "a12", "a13", "a14", "a15", "a16", "a17", "a18", "a19", "a20", "a21", "a22", "a23", "a24", "a25", "a26", "a27", "a28", "a29", "a30", "a31", "a32", "a33", "a34", "a35", "a36", "a37", "a38", "a39", "a40", "a41", "a42", "a43", "a44", "a45", "a46", "a47", "a48", "a49", "a50", "a51", "a52", "a53", "a54", "a55", "a56", "a57", "a58", "a59", "a60", "a61", "a62", "a63", "a64", "a65", "a66", "a67", "a68", "a69", "a70", "a71", "a72", "a73", "a74", "a75", "a76", "a77", "a78", "a79", "a80", "a81", "a82", "a83", "a84", "a85", "a86", "a87", "a88", "a89", "a90", "a91", "a92", "a93", "a94", "a95", "a96", "a97", "a98", "a99", "a100", "a101", "a102", "a103", "a104", "a105", "a106", "a107", "a108", "a109", "a110", "a111", "a112", "a113", "a114", "a115", "a116", "a117", "a118", "a119", "a120", "a121", "a122", "a123", "a124", "a125", "a126", "a127", "a128", "a129", "a130", "a131", "a132", "a133", "a134", "a135", "a136", "a137", "a138", "a139", "a140", "a141", "a142", "a143", "a144", "a145", "a146", "a147", "a148", "a149", "a150", "a151", "a152", "a153", "a154", "a155", "a156", "a157", "a158", "a159", "a160", "a161", "a162", "a163", "a164", "a165", "a166", "a167", "a168", "a169", "a170", "a171", "a172", "a173", "a174", "a175", "a176", "a177", "a178", "a179", "a180", "a181", "a182", "a183", "a184", "a185", "a186", "a187", "a188", "a189", "a190", "a191", "a192", "a193", "a194", "a195", "a196", "a197", "a198", "a199", "a200", "a201", "a202", "a203", "a204", "a205", "a206", "a207", "a208", "a209", "a210", "a211", "a212", "a213", "a214", "a215", "a216", "a217", "a218", "a219", "a220", "a221", "a222", "a223", "a224", "a225", "a226", "a227", "a228", "a229", "a230", "a231", "a232", "a233", "a234", "a235", "a236", "a237", "a238", "a239");
  v2u res[2][3];
  v4u st0, st1;
  v2u tag;
  f32x4 cq[3], cg[3];
  {
    FRESH();
    const v2u e = __builtin_amdgcn_raw_buffer_load_b64(rsW, 0, EPOCH_OFF, 16);
    v4u va[8], vb[8];
    prep_load(b, W, va, tid, w);
    prep_load(256 + b, W, vb, tid, w);
    asm volatile("" ::: "memory");
    prep_store(b, va, rsW, lds + LDS_R1, tid, w);
    {
      const unsigned e0 = __builtin_amdgcn_readfirstlane(e[0]), e1 = __builtin_amdgcn_readfirstlane(e[1]);
      tag = v2u{e0 + 1u, e1 + (e0 == 0xffffffffu ? 1u : 0u)};
    }
    vmcnt<0>();
    BAR();
    if (w == 0) __builtin_amdgcn_raw_buffer_store_b64(tag, rsW, 0, RF_OFF + b * 8, 16);
    prep_store(256 + b, vb, rsW, lds + LDS_R1, tid, w);
#pragma unroll
    for (int s = 0; s < 2; ++s)
#pragma unroll
      for (int i = 0; i < 3; ++i)
        res[s][i] = __builtin_amdgcn_raw_buffer_load_b64(rsX, (unsigned)(r * 1280 + gq * 8),
                                                         (unsigned)((128 * p + 64 * s + 32 * rh + 16 * (w & 1)) * 1280 + 160 * c + 32 * ((i + 3 * (w >> 1)) % 5)), 0);
    v2u fv = poll_issue(rsW, lane * 8, RF_OFF + w * 512);
    poll_check<0>(fv, tag, rsW, lane * 8, RF_OFF + w * 512);
    BAR();
    if (w == 0) __builtin_amdgcn_raw_buffer_store_b64(tag, rsW, 0, RF_OFF + 2048 + b * 8, 16);
    if (b == 0 && w == 1) __builtin_amdgcn_raw_buffer_store_b64(tag, rsW, 0, EPOCH_OFF, 16);
    unsigned wb = WQ_OFF + (g * 160 + w) * 1024; OPAQUE(wb);
    st0 = ldc_asm(rsW, tid * 32, STX_OFF + p * 16384); st1 = ldc_asm(rsW, tid * 32 + 16, STX_OFF + p * 16384);
    unsigned ab = X0_OFF + p * 163840 + w * 1024, lb = LDS_R0 + w * 1024; OPAQUE(ab); OPAQUE(lb);
    sfor<20>([&](auto I) { ldw<2 * I.value>(rsW, l16, wb + 2 * I.value * 4096); ldw<2 * I.value + 1>(rsW, l16, wb + (2 * I.value + 1) * 4096); dma2<I.value * 4096>(rsW, l16, ab + I.value * 4096, lb); });
  }

  float sm_, sq_;
  auto stats_stage = [&](auto STc, int sub, int tid) {
    constexpr int ST = STc.value;
    if constexpr (ST == 0) {
      asm volatile("" : "+v"(st0), "+v"(st1));
      f32x4 fa = __builtin_bit_cast(f32x4, st0) + __builtin_bit_cast(f32x4, st1);
      sm_ = fa[0] + fa[2]; sq_ = fa[1] + fa[3];
    } else if constexpr (ST == 1) { sm_ += dpp_xor1(sm_); sq_ += dpp_xor1(sq_); }
    else if constexpr (ST == 2) { sm_ += dpp_xor2(sm_); sq_ += dpp_xor2(sq_); }
    else if constexpr (ST == 3) { sm_ *= (1.f / 640.f); sq_ = fmaxf(sq_ * (1.f / 640.f) - sm_ * sm_, 0.f); sq_ = rbf(sq_); }
    else if constexpr (ST == 4) { sq_ = rbf(__builtin_amdgcn_sqrtf(rbf(sq_ + 1e-5f))); }
    else { if ((tid & 3) == 0) *(f32x2*)(lds + LDS_ST + (sub * 64 + (tid >> 2)) * 8) = f32x2{rbf(sm_), __builtin_amdgcn_rcpf(sq_)}; }
  };
  auto ring_gemm = [&](auto NJc, auto SUBc, auto NAFTERc, const Sched& dummy, auto SCH, f32x4 (&acc)[3][4], f32x4 (&c4)[3], bool do_cs, unsigned l16, auto&& bg) {
    constexpr int NJ = NJc.value, SUB = SUBc.value, NAFTER = NAFTERc.value;
    constexpr const Sched& S = SCH.get();
    unsigned rb0 = (SUB == 0 ? LDS_R0 : LDS_R1) + l16, rb1 = rb0 + 65536, rbx = LDS_R1X + l16;
    asm volatile("" : "+v"(rb0), "+v"(rb1), "+v"(rbx));
    auto rd = [&](int ks, int m) -> v4u {
      if (SUB == 1 && ks == 19) return *(const LAS v4u*)(rbx + m * 1024);
      return ks < 16 ? *(const LAS v4u*)(rb0 + ks * 4096 + m * 1024) : *(const LAS v4u*)(rb1 + (ks - 16) * 4096 + m * 1024);
    };
    auto gend = [](int k) constexpr -> int {
      if (SUB == 0) return k == 0 ? 1 : (k == 2 ? 3 : ((k % 4 == 0) ? k + 3 : -1));
      return k == 0 ? 9 : (k == 10 ? 19 : -1);
    };
    v4u xa[3][4];
    v4u ones = {0x3f803f80u, 0x3f803f80u, 0x3f803f80u, 0x3f803f80u};
    asm volatile("" : "+v"(ones));
    vmcnt<19 - gend(0) + NAFTER>();
    BAR();
#pragma unroll
    for (int k = 0; k < 2; ++k)
#pragma unroll
      for (int m = 0; m < 4; ++m) xa[k][m] = rd(k, m);
    SBAR();
    sfor<20>([&](auto KS) {
      constexpr int ks = KS.value;
      if constexpr (ks + 2 < 20 && gend(ks + 2) >= 0) { vmcnt<19 - gend(ks + 2) + NAFTER + ops_before(S, ks)>(); asm volatile("s_barrier" ::: "memory"); }
      if constexpr (ks == 18) BAR();
      if constexpr (SUB == 0) {
        if (do_cs) sfor<NJ>([&](auto J) { if constexpr (ks == 0) mfma_w0<J.value>(c4[J.value], ones); else mfma_w<ks * NJ + J.value>(c4[J.value], ones); });
        SBAR();
      }
      sfor<4>([&](auto M) {
        constexpr int m = M.value;
        sfor<NJ>([&](auto J) {
          constexpr int j = J.value;
          if constexpr (ks == 0) mfma_w0<j>(acc[j][m], xa[0][m]); else mfma_w<ks * NJ + j>(acc[j][m], xa[ks % 3][m]);
          if constexpr (j == 0 && ks < 18) xa[(ks + 2) % 3][m] = rd(ks + 2, m);
          if constexpr (j == 1 && S.op[ks][m][0] != 0) bg(IC<S.op[ks][m][0]>{}, KS, M);
          if constexpr (j == NJ - 1 && S.op[ks][m][1] != 0) bg(IC<S.op[ks][m][1]>{}, KS, M);
          SBAR();
        });
      });
    });
    MFMA_DRAIN();
  };

#pragma unroll 1
  for (int layer = 0; layer < 2; ++layer) {
    const unsigned LB = ACT_OFF + layer * LAYER_BYTES;
    {
      FRESH();
      STAMP(0);
      f32x4 (&c4)[3] = cq;
      unsigned abase = (layer == 0 ? X0_OFF : ACT_OFF + L_X3) + p * 163840 + 81920 + w * 1024;
      const unsigned stb = (layer == 0 ? STX_OFF : ACT_OFF + L_S3) + p * 16384 + 8192;
      unsigned lb1 = LDS_R1 + w * 1024, wob = WO_OFF + (c * 100 + w) * 1024, wgb = WG_OFF + (g * 240 + w) * 1024;
      OPAQUE(abase); OPAQUE(lb1); OPAQUE(wob); OPAQUE(wgb);
      f32x4 acc[3][4];
      v2u fv;
      f32x2 st[4]; v4u sv[3]; v2u f4[4];
      auto epilogue1 = [&](auto SUBc, auto&& mid) {
        constexpr int SUB = SUBc.value;
        constexpr int KVO = (SUB == 0 ? LDS_KVA : LDS_KVB) + 8 * TP;
        if (tid < 8) *(v4u*)(lds + KVO + (tid >> 1) * TP + 2560 + (tid & 1) * 16) = v4u{(tid & 1) ? 0u : 0x00003f80u, 0, 0, 0};
        mid(IC<0>{});
        SBAR();
        sfor<2>([&](auto J) {
          constexpr int j = J.value;
          const int pc0 = 16 * (2 * w + j) + 4 * gq;
          const int a0 = pc0 < 40 ? LDS_Q + (SUB * 64 + r) * 80 + pc0 * 2 : (pc0 < 120 ? KVO + r * 160 + (pc0 - 40) * 2 : KVO + 2688 + r * 8);
          const int ts = pc0 < 40 ? 1280 : TP;
          const f32x2 ca = {c4[j][0], c4[j][1]}, cb = {c4[j][2], c4[j][3]};
          sfor<4>([&](auto M) {
            constexpr int m = M.value;
            const f32x2 nm = {-st[m][0], -st[m][0]}, rs2 = {st[m][1], st[m][1]};
            f32x2 ya = __builtin_elementwise_fma(nm, ca, f32x2{acc[j][m][0], acc[j][m][1]}) * rs2;
            f32x2 yb = __builtin_elementwise_fma(nm, cb, f32x2{acc[j][m][2], acc[j][m][3]}) * rs2;
            *(v2u*)(lds + a0 + m * ts) = v2u{pk2(ya[0], ya[1]), pk2(yb[0], yb[1])};
          });
          sfor<4>([&](auto M) { mid(IC<1 + j * 4 + M.value>{}); });
          SBAR();
        });
      };
      struct G1 { static constexpr const Sched& get() { return SS1; } };
      ring_gemm(IC<2>{}, IC<0>{}, IC<0>{}, SS1, G1{}, acc, cq, layer == 0, l16, [&](auto OP, auto KS, auto M) {
        constexpr int op = OP.value;
        if constexpr (op >= 2000 && op < 3000) ldw<40 + op - 2000>(rsW, l16, rot5<op - 2000>(wob, w >> 1));
        else if constexpr (op >= 1000 && op < 2000) dma2<(op - 1000) * 4096>(rsW, l16, abase + (op - 1000) * 4096, lb1);
        else if constexpr (op == 3001) fv = poll_issue(rsW, lane * 8, flag_off(0, PH_X3, p, 1));
        else if constexpr (op == 3002) poll_check<ops_after_code(SS1, 3001, KS.value, M.value)>(fv, tag, rsW, lane * 8, flag_off(0, PH_X3, p, 1), layer > 0);
        else if constexpr (op == 3006) sfor<4>([&](auto K) { f4[K.value] = poll_issue(rsW, lane * 8, RF_OFF + 2048 + K.value * 512); });
        else if constexpr (op == 3007) sfor<4>([&](auto K) { poll_check<0>(f4[K.value], tag, rsW, lane * 8, RF_OFF + 2048 + K.value * 512, layer == 0); });
        else if constexpr (op == 3003) { st0 = ldc_asm(rsW, tid * 32, stb); st1 = ldc_asm(rsW, tid * 32 + 16, stb); }
        else if constexpr (op >= 3010 && op < 3016) stats_stage(IC<op - 3010>{}, 0, tid);
        else if constexpr (op == 3030) {
#pragma unroll
          for (int m = 0; m < 4; ++m) st[m] = *(const f32x2*)(lds + LDS_ST + (m * 16 + r) * 8);
        }
      });
      STAMP(1);
      epilogue1(IC<0>{}, [&](auto I) {
        constexpr int i = I.value;
        if constexpr (i == 0) {
        } else if constexpr (i <= 6) { dma2<(5 + 2 * i) * 4096>(rsW, l16, abase + (5 + 2 * i) * 4096, lb1); dma2<(6 + 2 * i) * 4096>(rsW, l16, abase + (6 + 2 * i) * 4096, lb1); }
        else if constexpr (i == 7) dma2<LDS_R1X - LDS_R1>(rsW, l16, abase + 19 * 4096, lb1);
      });
      STAMP(2);
      const unsigned kvf_v = (unsigned)((((lane >> 2) & 1) && p >= 2 ? 1024 : 0) + (g * 4 + (lane & 3)) * 8);
      const unsigned kvf_s = flag_off(layer, PH_KV, p >= 2 ? p - 2 : 0, 0);
      unsigned kb1 = (p >= 1 ? LB + L_KV + ((p - 1) * 16 + g) * 24576 : NULL_OFF) + w * 1024, kb2 = (p >= 2 ? LB + L_KV + ((p - 2) * 16 + g) * 24576 : NULL_OFF) + w * 1024, lkb = w * 1024;
      OPAQUE(kb1); OPAQUE(kb2); OPAQUE(lkb);
      struct G2 { static constexpr const Sched& get() { return SS2; } };
      ring_gemm(IC<2>{}, IC<1>{}, IC<0>{}, SS2, G2{}, acc, cq, false, l16, [&](auto OP, auto KS, auto M) {
        constexpr int op = OP.value;
        if constexpr (op >= 2000 && op < 3000) ldw<op - 2000>(rsW, l16, wgb + (op - 2000) * 4096);
        else if constexpr (op == 3000) set_flag<ops_after_code(SS2, 3023, KS.value, M.value)>(tag, rsW, flag_off(layer, PH_KV, p, 0) + (g * 4 + w) * 8);
        else if constexpr (op == 3001) fv = poll_issue(rsW, kvf_v, kvf_s);
        else if constexpr (op == 3002) poll_check<ops_after_code(SS2, 3001, KS.value, M.value)>(fv, tag, rsW, kvf_v, kvf_s, p > 0);
        else if constexpr (op >= 4000 && op < 4003) dma2<LDS_KVA + 4 * TP + (op - 4000) * 4096>(rsW, l16, kb1 + (op - 4000) * 4096, lkb);
        else if constexpr (op >= 4003 && op < 4006) dma2<LDS_KVA + (op - 4003) * 4096>(rsW, l16, kb2 + (op - 4003) * 4096, lkb);
        else if constexpr (op >= 3010 && op < 3016) stats_stage(IC<op - 3010>{}, 1, tid);
        else if constexpr (op == 3020) {
#pragma unroll
          for (int i = 0; i < 3; ++i) sv[i] = *(const v4u*)(lds + LDS_KVA + 8 * TP + (tid + 256 * i) * 16);
        } else if constexpr (op > 3020 && op <= 3023) { stc(sv[op - 3021], rsW, (unsigned)(tid + 256 * (op - 3021)) * 16, LB + L_KV + (p * 16 + g) * 24576); asm volatile("" ::: "memory"); }
        else if constexpr (op == 3030) {
#pragma unroll
          for (int m = 0; m < 4; ++m) st[m] = *(const f32x2*)(lds + LDS_ST + (64 + m * 16 + r) * 8);
        }
      });
      STAMP(3);
      epilogue1(IC<1>{}, [&](auto) {});
      STAMP(4);
      vmcnt<0>();
      BAR();
    }
    v4u af[24];
    {
      FRESH();
      unsigned wob = WO_OFF + (c * 100 + w) * 1024; OPAQUE(wob);
      const unsigned kvf_v = (unsigned)((((lane >> 2) & 1) && p >= 2 ? 1024 : 0) + (g * 4 + (lane & 3)) * 8);
      const unsigned kvf_s = flag_off(layer, PH_KV, p >= 2 ? p - 2 : 0, 1);
      unsigned kb1 = (p >= 1 ? LB + L_KV + ((p - 1) * 16 + g) * 24576 + 12288 : NULL_OFF) + w * 1024, kb2 = (p >= 2 ? LB + L_KV + ((p - 2) * 16 + g) * 24576 + 12288 : NULL_OFF) + w * 1024, lkb = w * 1024;
      OPAQUE(kb1); OPAQUE(kb2); OPAQUE(lkb);
      const int hh = w & 1, jb = (w >> 1) * 2;
      const float SC = 0.22360679774997896f * 1.4426950408889634f;
      const int q4 = (lane & 15) >> 2, p4 = lane & 3;
      const unsigned koff = gq < 3 ? r * 160 + hh * 80 + 16 * gq : 2576;
      const unsigned voff0 = (4 * gq + q4) * 160 + hh * 80 + 40 + 8 * p4;
      const unsigned voff1 = p4 == 0 ? (4 * gq + q4) * 160 + hh * 80 + 40 + 32 : (p4 == 1 ? 2560 : 2568);
      bool up[4];
#pragma unroll
      for (int e = 0; e < 4; ++e) up[e] = (4 * gq + e) > r;
      const unsigned mA0 = (up[0] ? 0xffffu : 0u) | (up[1] ? 0xffff0000u : 0u), mA1 = (up[2] ? 0xffffu : 0u) | (up[3] ? 0xffff0000u : 0u);
      f32x4 o0[4], o1[4]; float mx[4];
      v2u fv;
      v4u kf[10]; v4u qf[2]; v2u qlo[2], qhi[2];
      auto dbase = [&](int D) -> const char* { return lds + (D < 2 ? LDS_KVA : LDS_KVB) + ((D == 0 || D == 3) ? jb : (D == 1 ? 4 : 0)) * TP; };
      auto qrow_of = [&](int D, int u) -> char* { return lds + LDS_Q + (16 * ((D & 1) * 4 + jb + u) + r) * 80 + hh * 40; };
      auto read_q = [&](int D) {
#pragma unroll
        for (int u = 0; u < 2; ++u) {
          qlo[u] = *(const v2u*)(qrow_of(D, u) + (gq < 2 ? 16 * gq : 32));
          qhi[u] = *(const v2u*)(qrow_of(D, u) + (gq < 2 ? 16 * gq + 8 : 32));
        }
      };
      auto make_q = [&]() {
#pragma unroll
        for (int u = 0; u < 2; ++u) qf[u] = v4u{gq < 3 ? qlo[u][0] : 0x3f80u, gq < 3 ? qlo[u][1] : 0u, gq < 2 ? qhi[u][0] : 0u, gq < 2 ? qhi[u][1] : 0u};
        asm volatile("" : "+v"(qf[0]), "+v"(qf[1]));
      };
      auto read_k = [&](int D, int t) { kf[t] = *(const v4u*)(dbase(D) + t * TP + koff); };
      auto load_kq = [&](auto Dc) {
        constexpr int D = Dc.value;
        read_q(D);
#pragma unroll
        for (int t = 0; t < ((D == 0 || D == 3) ? 10 : 8); ++t) read_k(D, t);
        make_q();
        SBAR();
      };
      auto dchunk = [&](auto Dc, auto PIPEc, auto&& bg) {
        constexpr int D = Dc.value, UB = (D & 1) * 2; constexpr bool MASKED = D == 0 || D == 3, FIRST = D < 2, PIPE = PIPEc.value != 0;
        constexpr int NT = MASKED ? 9 : 8, NP = MASKED ? 5 : 4, NKN = (D + 1 == 3) ? 10 : 8;
        f32x4 s[2][9]; v4u vq0[5], vq1[5];
        const char* kvb = dbase(D);
        auto read_v = [&](int pr, int hf) {
          const v2u a = __builtin_bit_cast(v2u, __builtin_amdgcn_ds_read_tr16_b64_v4i16((LAS s16x4*)(kvb + 2 * pr * TP + (hf ? voff1 : voff0))));
          const v2u bq = __builtin_bit_cast(v2u, __builtin_amdgcn_ds_read_tr16_b64_v4i16((LAS s16x4*)(kvb + (2 * pr + 1) * TP + (hf ? voff1 : voff0))));
          (hf ? vq1 : vq0)[pr] = v4u{a[0], a[1], bq[0], bq[1]};
        };
        float ma[2], mb[2];
        auto tord = [](int i) constexpr -> int { return MASKED ? (i == 0 ? 0 : (i == 1 ? 8 : i - 1)) : i; };
        auto maxstep = [&](auto Ic) {
          constexpr int i = Ic.value;
#pragma unroll
          for (int u = 0; u < 2; ++u) {
            if constexpr (MASKED && i == 0) {}
            else if constexpr (MASKED && i == 1) {
#pragma unroll
              for (int e = 0; e < 4; ++e) s[u][0][e] = up[e] ? s[u][0][e] : s[u][8][e];
              ma[u] = max3(s[u][0][0], s[u][0][1], s[u][0][1]); mb[u] = max3(s[u][0][2], s[u][0][3], s[u][0][3]);
            } else if constexpr (i == 0) { ma[u] = max3(s[u][0][0], s[u][0][1], s[u][0][1]); mb[u] = max3(s[u][0][2], s[u][0][3], s[u][0][3]); }
            else { constexpr int t = tord(i); ma[u] = max3(ma[u], s[u][t][0], s[u][t][1]); mb[u] = max3(mb[u], s[u][t][2], s[u][t][3]); }
          }
        };
        asm volatile("s_nop 1" ::: "memory");
        sfor<NT>([&](auto I) {
          constexpr int i = I.value, t = tord(i);
          mfma_n0(s[0][t], kf[t], qf[0]);
          if constexpr (i < 2 * NP) read_v(i >> 1, i & 1);
          SBAR();
          mfma_n0(s[1][t], kf[MASKED ? t + 1 : t], qf[1]);
          if constexpr (i >= 2) maxstep(IC<i - 2>{});
          SBAR();
        });
        if constexpr (MASKED) read_v(4, 1);
        asm volatile("s_nop 7" ::: "memory");
        SBAR();
        maxstep(IC<NT - 2>{});
        asm volatile("s_nop 3" ::: "memory");
        SBAR();
        maxstep(IC<NT - 1>{});
        SBAR();
        bg(IC<0>{});
        if constexpr (PIPE) read_q(D + 1);
        float mc[2];
#pragma unroll
        for (int u = 0; u < 2; ++u) {
          const float m = max1632(FIRST ? max3(ma[u], mb[u], mb[u]) : max3(ma[u], mb[u], mx[UB + u]));
          mc[u] = -m * SC;
          if constexpr (!FIRST) {
            const float al = __builtin_amdgcn_exp2f(fmaf(mx[UB + u], SC, mc[u]));
            o0[UB + u] *= al; o1[UB + u] *= al;
          }
          mx[UB + u] = m;
        }
        const f32x2 sc2 = {SC, SC};
#pragma unroll
        for (int t = 0; t < 8; ++t)
#pragma unroll
          for (int u = 0; u < 2; ++u) {
            const f32x2 za = __builtin_elementwise_fma(f32x2{s[u][t][0], s[u][t][1]}, sc2, f32x2{mc[u], mc[u]});
            const f32x2 zb = __builtin_elementwise_fma(f32x2{s[u][t][2], s[u][t][3]}, sc2, f32x2{mc[u], mc[u]});
            s[u][t] = f32x4{za[0], za[1], zb[0], zb[1]};
          }
        SBAR();
        unsigned pp[2][8][2], pe[2][4];
        auto pv = [&](auto PRc, auto Uc, auto Hc, auto F0c) {
          constexpr int pr = PRc.value, u = Uc.value, hsel = Hc.value;
          v4u pf;
          if constexpr (!MASKED) pf = v4u{pp[u][2 * pr][0], pp[u][2 * pr][1], pp[u][2 * pr + 1][0], pp[u][2 * pr + 1][1]};
          else if constexpr (u == 0) {
            if constexpr (pr == 0) pf = v4u{pe[0][0], pe[0][1], pp[0][1][0], pp[0][1][1]};
            else if constexpr (pr == 4) pf = v4u{pe[0][2], pe[0][3], 0u, 0u};
            else pf = v4u{pp[0][2 * pr][0], pp[0][2 * pr][1], pp[0][2 * pr + 1][0], pp[0][2 * pr + 1][1]};
          } else {
            if constexpr (pr == 0) pf = v4u{0u, 0u, pe[1][0], pe[1][1]};
            else if constexpr (pr == 4) pf = v4u{pp[1][7][0], pp[1][7][1], pe[1][2], pe[1][3]};
            else pf = v4u{pp[1][2 * pr - 1][0], pp[1][2 * pr - 1][1], pp[1][2 * pr][0], pp[1][2 * pr][1]};
          }
          f32x4& o = hsel == 0 ? o0[UB + u] : o1[UB + u];
          if constexpr (FIRST && F0c.value != 0) mfma_v0(o, hsel == 0 ? vq0[pr] : vq1[pr], pf); else mfma_v(o, hsel == 0 ? vq0[pr] : vq1[pr], pf);
        };
        auto rdy = [](int u, int t) constexpr -> int {
          if (!MASKED) return (t & 1) ? t >> 1 : -1;
          if (u == 0) return t == 0 ? 4 : ((t & 1) ? t >> 1 : -1);
          return t == 0 ? 0 : (t == 7 ? 4 : ((t & 1) == 0 ? t >> 1 : -1));
        };
        auto firstp = [](int u) constexpr -> int { return MASKED ? (u == 0 ? 4 : 0) : 0; };
        sfor<10>([&](auto T) {
          constexpr int t = T.value;
          sfor<2>([&](auto Uc) {
            constexpr int u = Uc.value;
            if constexpr (t < 8) { s[u][t][0] = __builtin_amdgcn_exp2f(s[u][t][0]); s[u][t][1] = __builtin_amdgcn_exp2f(s[u][t][1]); }
            SBAR();
            if constexpr (t >= 2 && rdy(u, t - 2) >= 0) pv(IC<rdy(u, t - 2)>{}, Uc, IC<0>{}, IC<rdy(u, t - 2) == firstp(u)>{});
            else if constexpr (PIPE && t < 8 && 2 * t + u < NKN && t < 5) read_k(D + 1, 2 * t + u);
            SBAR();
            if constexpr (t < 8) { s[u][t][2] = __builtin_amdgcn_exp2f(s[u][t][2]); s[u][t][3] = __builtin_amdgcn_exp2f(s[u][t][3]); }
            if constexpr (t >= 1 && t <= 8) {
              pp[u][t - 1][0] = pk2(s[u][t - 1][0], s[u][t - 1][1]); pp[u][t - 1][1] = pk2(s[u][t - 1][2], s[u][t - 1][3]);
              if constexpr (MASKED && t == 1) { pe[u][0] = pp[u][0][0] & mA0; pe[u][1] = pp[u][0][1] & mA1; pe[u][2] = pp[u][0][0] & ~mA0; pe[u][3] = pp[u][0][1] & ~mA1; }
            }
            SBAR();
            if constexpr (t >= 2 && rdy(u, t - 2) >= 0) {
              pv(IC<rdy(u, t - 2)>{}, Uc, IC<1>{}, IC<rdy(u, t - 2) == firstp(u)>{});
              if constexpr (PIPE && 2 * t + u < NKN && t < 5) read_k(D + 1, 2 * t + u);
            }
            SBAR();
          });
          if constexpr (t < 8) bg(IC<1 + t>{});
        });
        if constexpr (PIPE) make_q();
        SBAR();
        if constexpr (!FIRST) {
          MFMA_DRAIN();
#pragma unroll
          for (int u = 0; u < 2; ++u) {
            char* qrow = qrow_of(D, u);
            const float inv = __builtin_amdgcn_rcpf(bcast_row1(o1[UB + u][0]));
            *(v2u*)(qrow + 8 * gq) = v2u{pk2(o0[UB + u][0] * inv, o0[UB + u][1] * inv), pk2(o0[UB + u][2] * inv, o0[UB + u][3] * inv)};
            *(v2u*)(gq == 0 ? qrow + 32 : lds + LDS_KVB + 8 * TP + 2816 + lane * 2 - (lane & 3) * 2 + 0) = v2u{pk2(o1[UB + u][0] * inv, o1[UB + u][1] * inv), pk2(o1[UB + u][2] * inv, o1[UB + u][3] * inv)};
          }
        }
        SBAR();
      };
      auto nobg = [&](auto) {};
      auto store_o = [&](auto SUBc, auto&& mid) {
        constexpr int SUB = SUBc.value;
        BAR();
        v4u v[2];
        const int mt = (tid >> 4) & 3;
        v[0] = *(const v4u*)(lds + LDS_Q + (SUB * 64 + 16 * mt + r) * 80 + 16 * w);
        v[1] = *(const v4u*)(lds + LDS_Q + (SUB * 64 + 16 * mt + r) * 80 + 64);
        mid(IC<0>{});
        SBAR();
        const unsigned vo = (unsigned)((mt >> 1) * 40960 + (mt & 1) * 1024 + r * 16), ob = LB + L_O + p * 163840 + SUB * 81920;
        { const int c8 = 5 * g + w; stc(v[0], rsW, vo, ob + (c8 >> 2) * 2048 + (c8 & 3) * 256); }
        if (w == 0) { const int c8 = 5 * g + 4; stc(v[1], rsW, vo, ob + (c8 >> 2) * 2048 + (c8 & 3) * 256); }
        asm volatile("" ::: "memory");
      };
      {
        v4u sv[3];
#pragma unroll
        for (int i = 0; i < 3; ++i) sv[i] = *(const v4u*)(lds + LDS_KVB + 8 * TP + (tid + 256 * i) * 16);
        load_kq(IC<0>{});
#pragma unroll
        for (int i = 0; i < 3; ++i) stc(sv[i], rsW, (unsigned)(tid + 256 * i) * 16, LB + L_KV + (p * 16 + g) * 24576 + 12288);
        asm volatile("" ::: "memory");
      }
      dchunk(IC<0>{}, IC<1>{}, [&](auto I) {
        if constexpr (I.value == 2) set_flag<0>(tag, rsW, flag_off(layer, PH_KV, p, 1) + (g * 4 + w) * 8);
        if constexpr (I.value >= 3 && I.value <= 7) ldw<35 + I.value - 3>(rsW, l16, rot5<20 + I.value - 3>(wob, w >> 1));
      });
      dchunk(IC<1>{}, IC<0>{}, [&](auto I) {
        if constexpr (I.value == 0) fv = poll_issue(rsW, kvf_v, kvf_s);
        if constexpr (I.value == 4) {
          poll_check<0>(fv, tag, rsW, kvf_v, kvf_s, p > 0);
          sfor<3>([&](auto K) { dma2<LDS_KVB + 4 * TP + K.value * 4096>(rsW, l16, kb1 + K.value * 4096, lkb); });
        }
        if constexpr (I.value == 5) sfor<3>([&](auto K) { dma2<LDS_KVB + K.value * 4096>(rsW, l16, kb2 + K.value * 4096, lkb); });
      });
      STAMP(5);
      vmcnt<0>();
      BAR();
      STAMP(6);
      load_kq(IC<2>{});
      dchunk(IC<2>{}, IC<1>{}, nobg);
      store_o(IC<0>{}, nobg);
      STAMP(7);
      dchunk(IC<3>{}, IC<0>{}, [&](auto I) {
        if constexpr (I.value == 1) set_flag<0>(tag, rsW, flag_off(layer, PH_O, p, 0) + (g * 4 + w) * 8);
        if constexpr (I.value == 8) fv = poll_issue(rsW, lane * 8, flag_off(layer, PH_O, p, 0));
      });
      unsigned ob = LB + L_O + p * 163840 + rh * 40960 + w * 2048; OPAQUE(ob);
      store_o(IC<1>{}, [&](auto) {
        poll_check<0>(fv, tag, rsW, lane * 8, flag_off(layer, PH_O, p, 0));
#pragma unroll
        for (int i = 0; i < 5; ++i)
#pragma unroll
          for (int m = 0; m < 2; ++m) af[i * 2 + m] = ldc(rsW, l16 + (m ^ (w & 1)) * 1024, ob + i * 8192);
        asm volatile("" ::: "memory");
      });
      STAMP(8);
    }
    auto reduce_epilogue = [&](auto SUBc, f32x4 (&acc)[5][2], unsigned xoff, unsigned soff, bool last, int tid, int w, int p, int g, auto&& mid) {
      constexpr int SUB = SUBc.value;
      const int lane = tid & 63, r = tid & 15, gq = (tid >> 4) & 3, rh = g & 1, c = g >> 1, mo = w & 1, h = w >> 1;
      char* rb = lds + LDS_RED + lane * 16;
#pragma unroll
      for (int n = 0; n < 5; ++n) {
        const int np = (n + 3 * h) % 5;
        *(f32x4*)(rb + (((mo ^ 1) * 5 + np) * 4 + w) * 1024) = acc[n][1];
        if (n >= 2) *(f32x4*)(rb + ((mo * 5 + np) * 4 + w) * 1024) = acc[n][0];
      }
      mid(IC<0>{});
      BAR();
      f32x4 rd[3][3];
#pragma unroll
      for (int n = 0; n < 3; ++n)
#pragma unroll
        for (int q = 1; q < 4; ++q) rd[n][q - 1] = *(const f32x4*)(rb + ((mo * 5 + (n + 3 * h) % 5) * 4 + ((w + q) & 3)) * 1024);
      mid(IC<1>{});
      SBAR();
      float sm = 0.f, sq = 0.f;
#pragma unroll
      for (int n = 0; n < 3; ++n) {
        const f32x4 sv = (acc[n][0] + rd[n][0]) + (rd[n][1] + rd[n][2]);
        const unsigned a0 = pk2(sv[0], sv[1]), a1 = pk2(sv[2], sv[3]);
        const v2u o = {pk2(bf2f(res[SUB][n][0] & 0xffffu) + bf2f(a0 & 0xffffu), bf2f(res[SUB][n][0] >> 16) + bf2f(a0 >> 16)),
                       pk2(bf2f(res[SUB][n][1] & 0xffffu) + bf2f(a1 & 0xffffu), bf2f(res[SUB][n][1] >> 16) + bf2f(a1 >> 16))};
        res[SUB][n] = o;
        if (n < 2 || h == 0) {
          const int np = (n + 3 * h) % 5;
          sm = dot2(o[0], 0x3f803f80u, sm); sq = dot2(o[0], o[0], sq); sm = dot2(o[1], 0x3f803f80u, sm); sq = dot2(o[1], o[1], sq);
          if (!last) {
            const int c8 = 10 * c + 2 * np;
            stc2(o, rsW, (unsigned)((gq >> 1) * 256 + r * 16 + (gq & 1) * 8), xoff + SUB * 81920 + (2 * rh + mo) * 1024 + (c8 >> 2) * 4096 + (c8 & 3) * 256);
          } else {
            *(v2u*)(out + (size_t)(128 * p + 64 * SUB + 32 * rh + 16 * mo + r) * 640 + 80 * c + 16 * np + 4 * gq) = o;
          }
        }
      }
      if (!last) {
        sm = sum32(sum16(sm)); sq = sum32(sum16(sq));
        if (lane < 16) stc2(v2u{__builtin_bit_cast(unsigned, sm), __builtin_bit_cast(unsigned, sq)}, rsW, (unsigned)(r * 128),
                            soff + (64 * SUB + 32 * rh + 16 * mo) * 128 + (c * 2 + h) * 8);
      }
      asm volatile("" ::: "memory");
    };
    {
      FRESH();
      unsigned ob = LB + L_O + p * 163840 + rh * 40960 + w * 2048; OPAQUE(ob);
      unsigned wgb = WG_OFF + (g * 240 + w) * 1024; OPAQUE(wgb);
      v2u fv;
      STAMP(9);
      f32x4 acc[5][2];
      auto p3_mma = [&](auto OFFc, auto&& mid) {
        constexpr int OFF = OFFc.value;
        sfor<5>([&](auto I) {
          mid(I);
          sfor<2>([&](auto M) { sfor<5>([&](auto N) {
            constexpr int i = I.value, m = M.value, n = N.value;
            if constexpr (i == 0) mfma_w0<40 + n>(acc[n][m], af[OFF + m]); else mfma_w<(i < 4 ? 40 + i * 5 + n : 35 + n)>(acc[n][m], af[OFF + i * 2 + m]);
          }); });
        });
        MFMA_DRAIN();
      };
      p3_mma(IC<0>{}, [&](auto I) { if constexpr (I.value == 2) set_flag<0>(tag, rsW, flag_off(layer, PH_O, p, 1) + (g * 4 + w) * 8); });
      reduce_epilogue(IC<0>{}, acc, LB + L_X2 + p * 163840, LB + L_S2 + p * 16384, false, tid, w, p, g, [&](auto I) {
        if constexpr (I.value == 0) fv = poll_issue(rsW, lane * 8, flag_off(layer, PH_O, p, 1));
        if constexpr (I.value == 1) {
          poll_check<0>(fv, tag, rsW, lane * 8, flag_off(layer, PH_O, p, 1));
#pragma unroll
          for (int i = 0; i < 5; ++i)
#pragma unroll
            for (int m = 0; m < 2; ++m) af[10 + i * 2 + m] = ldc(rsW, l16 + (m ^ (w & 1)) * 1024, ob + 81920 + i * 8192);
        }
      });
      STAMP(10);
      BAR();
      p3_mma(IC<10>{}, [&](auto I) { if constexpr (I.value == 3) set_flag<0>(tag, rsW, flag_off(layer, PH_X2, p, 0) + (g * 4 + w) * 8); });
      reduce_epilogue(IC<1>{}, acc, LB + L_X2 + p * 163840, LB + L_S2 + p * 16384, false, tid, w, p, g, [&](auto I) {
        if constexpr (I.value == 0) sfor<GLATE0 / 2>([&](auto K) { ldw<35 + K.value>(rsW, l16, wgb + (35 + K.value) * 4096); });
        else sfor<GLATE0 / 2>([&](auto K) { ldw<35 + GLATE0 / 2 + K.value>(rsW, l16, wgb + (35 + GLATE0 / 2 + K.value) * 4096); });
      });
      STAMP(11);
      fv = poll_issue(rsW, lane * 8, flag_off(layer, PH_X2, p, 0));
      set_flag<1>(tag, rsW, flag_off(layer, PH_X2, p, 1) + (g * 4 + w) * 8);
      poll_check<0>(fv, tag, rsW, lane * 8, flag_off(layer, PH_X2, p, 0));
      STAMP(12);
      st0 = ldc_asm(rsW, tid * 32, LB + L_S2 + p * 16384); st1 = ldc_asm(rsW, tid * 32 + 16, LB + L_S2 + p * 16384);
      unsigned ab = LB + L_X2 + p * 163840 + w * 1024, lb = LDS_R0 + w * 1024; OPAQUE(ab); OPAQUE(lb);
      sfor<20>([&](auto I) { dma2<I.value * 4096>(rsW, l16, ab + I.value * 4096, lb); });
    }
    {
      FRESH();
      f32x4 (&c4)[3] = cg;
      unsigned abase = LB + L_X2 + p * 163840 + 81920 + w * 1024, lb1 = LDS_R1 + w * 1024;
      unsigned wgb = WG_OFF + (g * 240 + w) * 1024, wdb = WD_OFF + (c * 240 + w) * 1024;
      unsigned hb = LB + L_H + p * 393216 + rh * 98304 + w * 2048;
      OPAQUE(abase); OPAQUE(lb1); OPAQUE(wgb); OPAQUE(wdb); OPAQUE(hb);
      f32x4 acc[3][4];
      v2u fv;
      f32x2 st[4]; v4u sv[3];
      const int mt = (tid >> 4) & 3;
      const unsigned lm0 = l16 + (w & 1) * 1024, lm1 = l16 + ((w & 1) ^ 1) * 1024;
      auto epilogue4 = [&](auto SUBc, auto&& mid) {
        constexpr int SUB = SUBc.value;
        mid(IC<0>{});
        SBAR();
        sfor<3>([&](auto J) {
          constexpr int j = J.value;
          const f32x2 ca = {c4[j][0], c4[j][1]}, cb = {c4[j][2], c4[j][3]};
          sfor<4>([&](auto M) {
            constexpr int m = M.value;
            const f32x2 nm = {-st[m][0], -st[m][0]}, rs2 = {st[m][1], st[m][1]};
            const float rl = st[m][1] * -1.4426950408889634f;
            f32x2 ge = __builtin_elementwise_fma(nm, ca, f32x2{acc[j][m][0], acc[j][m][1]}) * f32x2{rl, rl};
            f32x2 uu = __builtin_elementwise_fma(nm, cb, f32x2{acc[j][m][2], acc[j][m][3]}) * rs2;
            f32x2 dn = f32x2{__builtin_amdgcn_exp2f(ge[0]), __builtin_amdgcn_exp2f(ge[1])} + f32x2{1.f, 1.f};
            f32x2 hv = uu * f32x2{__builtin_amdgcn_rcpf(dn[0]), __builtin_amdgcn_rcpf(dn[1])};
            *(unsigned*)(lds + LDS_H + SUB * 16384 + (m * 16 + r) * HP + (24 * w + 8 * j + 2 * gq) * 2) = pk2(hv[0], hv[1]);
          });
          sfor<4>([&](auto M) { mid(IC<1 + j * 4 + M.value>{}); });
          SBAR();
        });
      };
      struct G8 { static constexpr const Sched& get() { return SS8; } };
      ring_gemm(IC<3>{}, IC<0>{}, IC<0>{}, SS8, G8{}, acc, cg, layer == 0, l16, [&](auto OP, auto KS, auto M) {
        constexpr int op = OP.value;
        if constexpr (op >= 2000 && op < 3000) ldw<op - 2000>(rsW, l16, wgb + (op - 2000) * 4096);
        else if constexpr (op >= 1000 && op < 2000) dma2<(op - 1000) * 4096>(rsW, l16, abase + (op - 1000) * 4096, lb1);
        else if constexpr (op == 3001) fv = poll_issue(rsW, lane * 8, flag_off(layer, PH_X2, p, 1));
        else if constexpr (op == 3002) poll_check<ops_after_code(SS8, 3001, KS.value, M.value)>(fv, tag, rsW, lane * 8, flag_off(layer, PH_X2, p, 1));
        else if constexpr (op == 3003) { st0 = ldc_asm(rsW, tid * 32, LB + L_S2 + p * 16384 + 8192); st1 = ldc_asm(rsW, tid * 32 + 16, LB + L_S2 + p * 16384 + 8192); }
        else if constexpr (op >= 3010 && op < 3016) stats_stage(IC<op - 3010>{}, 0, tid);
        else if constexpr (op == 3030) {
#pragma unroll
          for (int m = 0; m < 4; ++m) st[m] = *(const f32x2*)(lds + LDS_ST + (m * 16 + r) * 8);
        }
      });
      STAMP(13);
      epilogue4(IC<0>{}, [&](auto I) { if constexpr (I.value == 0) dma2<LDS_R1X - LDS_R1>(rsW, l16, abase + 19 * 4096, lb1); });
      STAMP(14);
      struct G9 { static constexpr const Sched& get() { return SS9; } };
      ring_gemm(IC<3>{}, IC<1>{}, IC<0>{}, SS9, G9{}, acc, cg, false, l16, [&](auto OP, auto KS, auto M) {
        constexpr int op = OP.value;
        if constexpr (op >= 2000 && op < 3000) ldw<op - 2000>(rsW, l16, rot5<op - 2000>(wdb, w >> 1));
        else if constexpr (op == 3000) set_flag<ops_after_code(SS9, 3023, KS.value, M.value)>(tag, rsW, flag_off(layer, PH_H, p, 0) + (g * 4 + w) * 8);
        else if constexpr (op == 3001) fv = poll_issue(rsW, lane * 8, flag_off(layer, PH_H, p, 0));
        else if constexpr (op == 3002) poll_check<ops_after_code(SS9, 3001, KS.value, M.value)>(fv, tag, rsW, lane * 8, flag_off(layer, PH_H, p, 0));
        else if constexpr (op >= 3010 && op < 3016) stats_stage(IC<op - 3010>{}, 1, tid);
        else if constexpr (op == 3020) {
#pragma unroll
          for (int i = 0; i < 3; ++i) sv[i] = *(const v4u*)(lds + LDS_H + (16 * mt + r) * HP + 16 * (w + 4 * i));
        } else if constexpr (op > 3020 && op <= 3023) {
          stc(sv[op - 3021], rsW, (unsigned)((mt >> 1) * 98304 + (mt & 1) * 1024 + r * 16), LB + L_H + p * 393216 + (3 * g + op - 3021) * 2048 + w * 256);
          asm volatile("" ::: "memory");
        } else if constexpr (op == 3030) {
#pragma unroll
          for (int m = 0; m < 4; ++m) st[m] = *(const f32x2*)(lds + LDS_ST + (64 + m * 16 + r) * 8);
        }
        else if constexpr (op >= 5000) { constexpr int i = op - 5000; af[i] = ldc_asm(rsW, (i & 1) ? lm1 : lm0, hb + (i >> 1) * 8192); }
      });
      STAMP(15);
      epilogue4(IC<1>{}, [&](auto I) {
        constexpr int i = I.value;
        if constexpr (i >= 1 && i <= 8) { af[6 + 2 * i] = ldc_asm(rsW, lm0, hb + (3 + i) * 8192); af[7 + 2 * i] = ldc_asm(rsW, lm1, hb + (3 + i) * 8192); }
        else if constexpr (i >= 9) sfor<3>([&](auto K) { constexpr int f = NBD9 + (i - 9) * 3 + K.value; ldw<f>(rsW, l16, rot5<f>(wdb, w >> 1)); });
      });
      sfor<60 - NBD9 - 12>([&](auto K) { constexpr int f = NBD9 + 12 + K.value; ldw<f>(rsW, l16, rot5<f>(wdb, w >> 1)); });
      STAMP(16);
    }
    {
      FRESH();
      unsigned hb = LB + L_H + p * 393216 + 196608 + rh * 98304 + w * 2048; OPAQUE(hb);
      unsigned wqb = WQ_OFF + (g * 160 + w) * 1024; OPAQUE(wqb);
      f32x4 acc[5][2];
      v2u fv;
      asm volatile("s_waitcnt vmcnt(14)" : "+v"(af[0]), "+v"(af[1]), "+v"(af[2]), "+v"(af[3]), "+v"(af[4]), "+v"(af[5]), "+v"(af[6]), "+v"(af[7]), "+v"(af[8]), "+v"(af[9]), "+v"(af[10]), "+v"(af[11]),
                   "+v"(af[12]), "+v"(af[13]), "+v"(af[14]), "+v"(af[15]), "+v"(af[16]), "+v"(af[17]), "+v"(af[18]), "+v"(af[19]), "+v"(af[20]), "+v"(af[21]), "+v"(af[22]), "+v"(af[23]) :: "memory");
      BAR();
      v4u sv[3];
      const int mt = (tid >> 4) & 3;
      sfor<12>([&](auto I) {
        constexpr int i = I.value;
        if constexpr (i == 8) set_flag<0>(tag, rsW, flag_off(layer, PH_H, p, 1) + (g * 4 + w) * 8);
        sfor<2>([&](auto M) { sfor<5>([&](auto N) {
          constexpr int m = M.value, n = N.value;
          if constexpr (i == 0) mfma_w0<n>(acc[n][m], af[m]); else mfma_w<i * 5 + n>(acc[n][m], af[i * 2 + m]);
          if constexpr (i == 0 && m == 0 && n < 3) sv[n] = *(const v4u*)(lds + LDS_H + 16384 + (16 * mt + r) * HP + 16 * (w + 4 * n));
          if constexpr (i == 1 && m == 0 && n < 3) {
            stc(sv[n], rsW, (unsigned)((mt >> 1) * 98304 + (mt & 1) * 1024 + r * 16), LB + L_H + p * 393216 + 196608 + (3 * g + n) * 2048 + w * 256);
            asm volatile("" ::: "memory");
          }
          SBAR();
        }); });
      });
      MFMA_DRAIN();
      STAMP(17);
      reduce_epilogue(IC<0>{}, acc, LB + L_X3 + p * 163840, LB + L_S3 + p * 16384, layer == 1, tid, w, p, g, [&](auto I) {
        if constexpr (I.value == 1) fv = poll_issue(rsW, lane * 8, flag_off(layer, PH_H, p, 1));
      });
      STAMP(18);
      poll_check<0>(fv, tag, rsW, lane * 8, flag_off(layer, PH_H, p, 1));
      STAMP(19);
#pragma unroll
      for (int i = 0; i < 12; ++i)
#pragma unroll
        for (int m = 0; m < 2; ++m) af[i * 2 + m] = ldc(rsW, l16 + (m ^ (w & 1)) * 1024, hb + i * 8192);
      if (layer == 0) set_flag<24>(tag, rsW, flag_off(layer, PH_X3, p, 0) + (g * 4 + w) * 8);
      BAR();
      SBAR();
      sfor<12>([&](auto I) {
        constexpr int i = I.value;
        sfor<2>([&](auto M) {
          constexpr int m = M.value;
          sfor<5>([&](auto N) { constexpr int n = N.value; if constexpr (i == 0) mfma_w0<n>(acc[n][m], af[m]); else mfma_w<i * 5 + n>(acc[n][m], af[i * 2 + m]); });
          if constexpr (i >= 4) { constexpr int k = (i - 4) * 2 + m; ldw<k>(rsW, l16, wqb + k * 4096); }
          SBAR();
        });
      });
      MFMA_DRAIN();
      STAMP(20);
      reduce_epilogue(IC<1>{}, acc, LB + L_X3 + p * 163840, LB + L_S3 + p * 16384, layer == 1, tid, w, p, g, [&](auto I) {
        if (layer == 0) sfor<12>([&](auto K) { constexpr int k = 16 + I.value * 12 + K.value; ldw<k>(rsW, l16, wqb + k * 4096); });
      });
      STAMP(21);
      if (layer == 0) {
        fv = poll_issue(rsW, lane * 8, flag_off(0, PH_X3, p, 0));
        set_flag<1>(tag, rsW, flag_off(0, PH_X3, p, 1) + (g * 4 + w) * 8);
        poll_check<0>(fv, tag, rsW, lane * 8, flag_off(0, PH_X3, p, 0));
        st0 = ldc_asm(rsW, tid * 32, LB + L_S3 + p * 16384); st1 = ldc_asm(rsW, tid * 32 + 16, LB + L_S3 + p * 16384);
        unsigned ab = LB + L_X3 + p * 163840 + w * 1024, lb = LDS_R0 + w * 1024; OPAQUE(ab); OPAQUE(lb);
        sfor<20>([&](auto I) { dma2<I.value * 4096>(rsW, l16, ab + I.value * 4096, lb); });
      }
      STAMP(22);
    }
  }
}

extern "C" void kernel_launch(void* const* d_in, const int* in_sizes, int n_in, void* d_out, int out_size, void* d_ws,
                              size_t ws_size, hipStream_t stream) {
  const u16* x = (const u16*)d_in[0];
  (void)hipFuncSetAttribute((const void*)k_main, hipFuncAttributeMaxDynamicSharedMemorySize, LDS_BYTES);
  Wts W{x, (const u16*)d_in[1], (const u16*)d_in[2], (const u16*)d_in[4], (const u16*)d_in[5], (const u16*)d_in[6]};
  k_main<<<256, 256, LDS_BYTES, stream>>>(W, (u16*)d_out, (char*)d_ws);
}
```

```cpp
#include <hip/hip_runtime.h>
#include <hip/hip_bf16.h>
#include <stdint.h>
#include <utility>

typedef unsigned short u16;
typedef unsigned v4u __attribute__((ext_vector_type(4)));
typedef unsigned v2u __attribute__((ext_vector_type(2)));
typedef float f32x4 __attribute__((ext_vector_type(4)));
typedef float f32x2 __attribute__((ext_vector_type(2)));
typedef short s16x4 __attribute__((ext_vector_type(4)));
typedef __bf16 bf16x2 __attribute__((ext_vector_type(2)));
typedef __bf16 bf16x8 __attribute__((ext_vector_type(8)));
#define GAS __attribute__((address_space(1)))
#define LAS __attribute__((address_space(3)))
#define DI __device__ __forceinline__
#define RLX_AGENT __ATOMIC_RELAXED, __HIP_MEMORY_SCOPE_AGENT
typedef GAS unsigned long long gu64;

template <int I> struct IC { static constexpr int value = I; };
template <class F, int... I> DI void sfor_(F&& f, std::integer_sequence<int, I...>) { (f(IC<I>{}), ...); }
template <int N, class F> DI void sfor(F&& f) { sfor_(f, std::make_integer_sequence<int, N>{}); }

constexpr int S_ = 2048, D_ = 640, F_ = 1536;
constexpr unsigned FLAG_OFF = 0, FLAG_BYTES = 262144;
constexpr unsigned RF_OFF = 163840;
constexpr unsigned EPOCH_OFF = 172032;
constexpr unsigned WQ_OFF = FLAG_BYTES;
constexpr unsigned WO_OFF = WQ_OFF + 16 * 160 * 1024;
constexpr unsigned WG_OFF = WO_OFF + 8 * 100 * 1024;
constexpr unsigned WD_OFF = WG_OFF + 16 * 240 * 1024;
constexpr unsigned CS_OFF = WD_OFF + 8 * 240 * 1024;
constexpr unsigned NULL_OFF = CS_OFF + 20480;
constexpr unsigned STX_OFF = CS_OFF + 32768;
constexpr unsigned ST_BYTES = 2048 * 128;
constexpr unsigned X0_OFF = STX_OFF + ST_BYTES;
constexpr unsigned ACT_OFF = X0_OFF + 16 * 160 * 1024;
constexpr unsigned KV_BYTES = 16 * 16 * 24 * 1024;
constexpr unsigned XF_BYTES = 16 * 160 * 1024;
constexpr unsigned HF_BYTES = 16 * 384 * 1024;
constexpr unsigned L_KV = 0, L_O = L_KV + KV_BYTES, L_X2 = L_O + XF_BYTES, L_H = L_X2 + XF_BYTES, L_X3 = L_H + HF_BYTES,
                   L_S2 = L_X3 + XF_BYTES, L_S3 = L_S2 + ST_BYTES, LAYER_BYTES = L_S3 + ST_BYTES;
constexpr unsigned WS_USED = ACT_OFF + 2 * LAYER_BYTES;
constexpr unsigned STAMP_OFF = WS_USED;
enum { PH_KV = 0, PH_O = 1, PH_X2 = 2, PH_H = 3, PH_X3 = 4 };

constexpr int TP = 3072;
constexpr int LDS_KVA = 0, LDS_KVB = 12 * TP;
constexpr int LDS_Q = 24 * TP;
constexpr int LDS_R0 = 0;
constexpr int LDS_R1 = 83968;
constexpr int LDS_R1X = LDS_KVB + 8 * TP;
constexpr int LDS_RED = LDS_R1;
constexpr int RP = 336;
constexpr int LDS_H = 0;
constexpr int HP = 208;
constexpr int LDS_ST = LDS_R1 + 19 * 4096;
constexpr int LDS_BYTES = LDS_ST + 1024;

DI float bf2f(unsigned b16) { return __builtin_bit_cast(float, b16 << 16); }
DI unsigned pk2(float a, float b) {
  bf16x2 r; r[0] = (__bf16)a; r[1] = (__bf16)b; return __builtin_bit_cast(unsigned, r);
}
DI float rbf(float a) { return bf2f(pk2(a, 0.f) & 0xffffu); }
template <int SL> DI void mfma_w(f32x4& c, const v4u& xv) { asm volatile("v_mfma_f32_16x16x32_bf16 %0, a[%c2:%c3], %1, %0" : "+v"(c) : "v"(xv), "i"(4 * SL), "i"(4 * SL + 3)); }
template <int SL> DI void mfma_w0(f32x4& c, const v4u& xv) { asm volatile("v_mfma_f32_16x16x32_bf16 %0, a[%c2:%c3], %1, 0" : "=&v"(c) : "v"(xv), "i"(4 * SL), "i"(4 * SL + 3)); }
template <int SL> DI void ldw(__amdgpu_buffer_rsrc_t rs, unsigned voff, unsigned soff) {
  asm volatile("buffer_load_dwordx4 a[%c3:%c4], %0, %1, %2 offen sc1" :: "v"(voff), "s"(rs), "s"(soff), "i"(4 * SL), "i"(4 * SL + 3) : "memory");
}
DI void mfma_v(f32x4& c, const v4u& a, const v4u& b) { asm volatile("s_nop 1\n\tv_mfma_f32_16x16x32_bf16 %0, %1, %2, %0" : "+v"(c) : "v"(a), "v"(b)); }
DI void mfma_v0(f32x4& c, const v4u& a, const v4u& b) { asm volatile("s_nop 1\n\tv_mfma_f32_16x16x32_bf16 %0, %1, %2, 0" : "=&v"(c) : "v"(a), "v"(b)); }
DI void mfma_n(f32x4& c, const v4u& a, const v4u& b) { asm volatile("v_mfma_f32_16x16x32_bf16 %0, %1, %2, %0" : "+v"(c) : "v"(a), "v"(b)); }
DI void mfma_n0(f32x4& c, const v4u& a, const v4u& b) { asm volatile("v_mfma_f32_16x16x32_bf16 %0, %1, %2, 0" : "=&v"(c) : "v"(a), "v"(b)); }
#define MFMA_DRAIN() do { __builtin_amdgcn_sched_barrier(0); asm volatile("s_nop 7\n\ts_nop 7\n\ts_nop 3" ::: "memory"); __builtin_amdgcn_sched_barrier(0); } while (0)
DI float dot2(unsigned a, unsigned b, float c) {
  return __builtin_amdgcn_fdot2_f32_bf16(__builtin_bit_cast(bf16x2, a), __builtin_bit_cast(bf16x2, b), c, false);
}
DI float max3(float a, float b, float c) { float d; asm("v_max3_f32 %0, %1, %2, %3" : "=v"(d) : "v"(a), "v"(b), "v"(c)); return d; }
DI v4u ldb(__amdgpu_buffer_rsrc_t rs, unsigned voff, unsigned soff) { return __builtin_amdgcn_raw_buffer_load_b128(rs, voff, soff, 0); }
DI v4u ldc(__amdgpu_buffer_rsrc_t rs, unsigned voff, unsigned soff) { return __builtin_amdgcn_raw_buffer_load_b128(rs, voff, soff, 16); }
DI void stc(v4u v, __amdgpu_buffer_rsrc_t rs, unsigned voff, unsigned soff) { __builtin_amdgcn_raw_buffer_store_b128(v, rs, voff, soff, 16); }
DI void stc2(v2u v, __amdgpu_buffer_rsrc_t rs, unsigned voff, unsigned soff) { __builtin_amdgcn_raw_buffer_store_b64(v, rs, voff, soff, 16); }
DI void dma(__amdgpu_buffer_rsrc_t rs, unsigned voff, unsigned soff, unsigned lds) {
  unsigned keep;
  asm volatile("s_mov_b32 %0, m0\n\ts_mov_b32 m0, %4\n\ts_nop 0\n\tbuffer_load_dwordx4 %1, %2, %3 offen sc1 lds\n\ts_mov_b32 m0, %0"
               : "=&s"(keep) : "v"(voff), "s"(rs), "s"(soff), "s"(lds) : "memory");
}
template <int LOFF> DI void dma2(__amdgpu_buffer_rsrc_t rs, unsigned voff, unsigned soff, unsigned lbase) {
  asm volatile("s_add_u32 m0, %3, %4\n\ts_nop 0\n\tbuffer_load_dwordx4 %0, %1, %2 offen sc1 lds" :: "v"(voff), "s"(rs), "s"(soff), "s"(lbase), "i"(LOFF) : "memory", "scc");
}
DI float dpp_xor1(float v) { return __builtin_bit_cast(float, __builtin_amdgcn_update_dpp(0, __builtin_bit_cast(int, v), 0xB1, 0xf, 0xf, true)); }
DI float dpp_xor2(float v) { return __builtin_bit_cast(float, __builtin_amdgcn_update_dpp(0, __builtin_bit_cast(int, v), 0x4E, 0xf, 0xf, true)); }
#define OPAQUE(v) asm volatile("" : "+s"(v))
#define FRESH() int tid = tid0, w = w0, p = p0, g = g0; asm volatile("" : "+v"(tid)); asm volatile("" : "+s"(w), "+s"(p), "+s"(g)); const int lane = tid & 63, r = tid & 15, gq = (tid >> 4) & 3, rh = g & 1, c = g >> 1; const unsigned l16 = lane * 16; (void)r; (void)gq; (void)l16; (void)rh; (void)c
template <int N> DI void vmcnt() { asm volatile("s_waitcnt vmcnt(%0)" :: "i"(N > 63 ? 63 : N) : "memory"); }
#define BAR() asm volatile("s_waitcnt lgkmcnt(0)\n\ts_barrier" ::: "memory")
#define SBAR() __builtin_amdgcn_sched_barrier(0)
DI float sum16(float v) { auto t = __builtin_amdgcn_permlane16_swap(__builtin_bit_cast(unsigned, v), __builtin_bit_cast(unsigned, v), false, false);
  return __builtin_bit_cast(float, (unsigned)t[0]) + __builtin_bit_cast(float, (unsigned)t[1]); }
DI float sum32(float v) { auto t = __builtin_amdgcn_permlane32_swap(__builtin_bit_cast(unsigned, v), __builtin_bit_cast(unsigned, v), false, false);
  return __builtin_bit_cast(float, (unsigned)t[0]) + __builtin_bit_cast(float, (unsigned)t[1]); }
DI float max1632(float v) {
  auto t = __builtin_amdgcn_permlane16_swap(__builtin_bit_cast(unsigned, v), __builtin_bit_cast(unsigned, v), false, false);
  float a = __builtin_bit_cast(float, (unsigned)t[0]), b = __builtin_bit_cast(float, (unsigned)t[1]);
  float m = max3(a, b, b);
  auto u = __builtin_amdgcn_permlane32_swap(__builtin_bit_cast(unsigned, m), __builtin_bit_cast(unsigned, m), false, false);
  float c = __builtin_bit_cast(float, (unsigned)u[0]), d = __builtin_bit_cast(float, (unsigned)u[1]);
  return max3(c, d, d);
}
DI float bcast_row1(float v) {
  auto t = __builtin_amdgcn_permlane16_swap(__builtin_bit_cast(unsigned, v), __builtin_bit_cast(unsigned, v), false, false);
  auto u = __builtin_amdgcn_permlane32_swap((unsigned)t[1], (unsigned)t[1], false, false);
  return __builtin_bit_cast(float, (unsigned)u[0]);
}

#define STAMP(i)

DI int qkv_src_col(int g, int pc) {
  if (pc < 40) return 40 * g + pc;
  if (pc < 60) return 640 + 40 * g + (pc - 40);
  if (pc < 80) return 1280 + 40 * g + (pc - 60);
  if (pc < 100) return 640 + 40 * g + 20 + (pc - 80);
  if (pc < 120) return 1280 + 40 * g + 20 + (pc - 100);
  return -1;
}
struct Wts { const u16 *x, *wqkv, *wo, *wg, *wu, *wd; };
DI void prep_load(int s, const Wts& W, v4u (&v)[8], int tid, int w) {
  const int lane = tid & 63;
  const u16* src = W.x; unsigned off = 0, step = 0;
  if (s < 80) {
    const int ch = lane < 60 ? lane : 0, sg = ch >= 40 ? 2 : (ch >= 20 ? 1 : 0);
    src = W.wqkv; off = (32 * (s >> 2) + w) * 1920 + sg * 640 + 160 * (s & 3) + (ch - 20 * sg) * 8; step = 4 * 1920;
  } else if (s < 208) {
    off = ((s - 80) * 16 + w * 4 + (lane >> 4)) * 640 + (lane & 15) * 8; step = 128;
  } else if (s < 368) {
    const int e0 = s - 208, ch = lane < 48 ? lane : 0;
    src = ch < 24 ? W.wg : W.wu; off = (32 * (e0 >> 3) + w) * 1536 + 192 * (e0 & 7) + (ch < 24 ? ch : ch - 24) * 8; step = 4 * 1536;
  } else if (s < 504) {
    const bool isd = s >= 408; const int e0 = isd ? s - 408 : s - 368, ch = lane < 40 ? lane : 0;
    src = isd ? W.wd : W.wo; off = (32 * (e0 >> 1) + w) * 640 + 320 * (e0 & 1) + ch * 8; step = 4 * 640;
  }
  const bool st = s >= 80 && s < 208;
#pragma unroll
  for (int i = 0; i < 8; ++i) v[i] = *(const GAS v4u*)(src + off + ((st && i >= 5) ? 0 : i) * step);
}
DI void prep_store(int s, const v4u (&v)[8], __amdgpu_buffer_rsrc_t rsW, char* sm, int tid, int w) {
  const int lane = tid & 63, gq = lane >> 4, q4 = (lane & 15) >> 2, p4 = lane & 3;
  auto emit = [&](unsigned ldsoff, int pitch, unsigned dst) {
    const char* a = sm + (8 * gq + q4) * pitch + ldsoff;
    v2u lo = __builtin_bit_cast(v2u, __builtin_amdgcn_ds_read_tr16_b64_v4i16((LAS s16x4*)a));
    v2u hi = __builtin_bit_cast(v2u, __builtin_amdgcn_ds_read_tr16_b64_v4i16((LAS s16x4*)(a + 4 * pitch)));
    stc(v4u{lo[0], lo[1], hi[0], hi[1]}, rsW, lane * 16, dst);
  };
  if (s < 80) {
    const int ks = s >> 2, g0 = (s & 3) * 4;
    constexpr int P = 976;
    if (lane <= 60) {
#pragma unroll
      for (int i = 0; i < 8; ++i) *(v4u*)(sm + (w + 4 * i) * P + lane * 16) = lane < 60 ? v[i] : v4u{0, 0, 0, 0};
    }
    BAR();
#pragma unroll
    for (int i = 0; i < 8; ++i) {
      const int pc = 16 * i + 4 * p4;
      const unsigned off = pc < 40 ? 80 * w + pc * 2 : (pc < 60 ? 320 + 80 * w + (pc - 40) * 2 : (pc < 80 ? 640 + 80 * w + (pc - 60) * 2 :
                           (pc < 100 ? 320 + 80 * w + 40 + (pc - 80) * 2 : (pc < 120 ? 640 + 80 * w + 40 + (pc - 100) * 2 : 960))));
      emit(off, P, WQ_OFF + (((g0 + w) * 20 + ks) * 8 + (i & 1) * 4 + (i >> 1)) * 1024);
    }
  } else if (s < 208) {
    const int row = (s - 80) * 16 + w * 4 + gq, r = lane & 15;
    float sm1 = 0.f, sq = 0.f;
#pragma unroll
    for (int i = 0; i < 5; ++i)
#pragma unroll
      for (int e = 0; e < 4; ++e) { sm1 = dot2(v[i][e], 0x3f803f80u, sm1); sq = dot2(v[i][e], v[i][e], sq); }
#pragma unroll
    for (int o = 8; o >= 1; o >>= 1) { sm1 += __shfl_xor(sm1, o); sq += __shfl_xor(sq, o); }
    stc2(r == 0 ? v2u{__builtin_bit_cast(unsigned, sm1), __builtin_bit_cast(unsigned, sq)} : v2u{0u, 0u}, rsW, STX_OFF + (row * 16 + r) * 8, 0);
    const int mt = s - 80;
#pragma unroll
    for (int i = 0; i < 5; ++i)
      stc(v[i], rsW, (unsigned)((r >> 2) * 4096 + (r & 3) * 256 + (w * 4 + gq) * 16), X0_OFF + (mt >> 2) * 81920 + (mt & 3) * 1024 + i * 16384);
  } else if (s < 368) {
    const int e0 = s - 208, ks = e0 >> 3, g0 = (e0 & 7) * 2;
    constexpr int P = 776;
    if (lane < 48) {
      char* d = sm + w * P + (lane < 24 ? lane * 32 : (lane - 24) * 32 + 4);
#pragma unroll
      for (int i = 0; i < 8; ++i)
#pragma unroll
        for (int e = 0; e < 4; ++e) *(unsigned*)(d + 4 * i * P + 8 * e) = v[i][e];
    }
    BAR();
#pragma unroll
    for (int i = 0; i < 6; ++i) {
      const int gl = w >> 1, wv = (w & 1) * 2 + i / 3, j = i % 3;
      emit((w * 6 + i) * 32 + p4 * 8, P, WG_OFF + (((g0 + gl) * 20 + ks) * 12 + j * 4 + wv) * 1024);
    }
  } else if (s < 504) {
    const bool isd = s >= 408; const int e0 = isd ? s - 408 : s - 368;
    const int ks = e0 >> 1, c = 4 * (e0 & 1) + w;
    constexpr int P = 648;
    if (lane < 40) {
#pragma unroll
      for (int i = 0; i < 8; ++i) {
        *(v2u*)(sm + (w + 4 * i) * P + lane * 16) = v2u{v[i][0], v[i][1]};
        *(v2u*)(sm + (w + 4 * i) * P + lane * 16 + 8) = v2u{v[i][2], v[i][3]};
      }
    }
    BAR();
    const unsigned dst = (isd ? WD_OFF + c * 240 * 1024 : WO_OFF + c * 100 * 1024) + ((ks >> 2) * 20 + (ks & 3)) * 1024;
#pragma unroll
    for (int i = 0; i < 5; ++i) emit((w * 80 + i * 16 + p4 * 4) * 2, P, dst + i * 4096);
  } else if (s == 504) {
#pragma unroll
    for (int i = 0; i < 3; ++i) { const int ch = i * 256 + tid, o = (ch * 16) % 3072; stc(v4u{o == 2560 ? 0x3f80u : (o == 2576 ? 0xF14Au : 0u), 0, 0, 0}, rsW, NULL_OFF + ch * 16, 0); }
  }
}

struct Sched { int op[20][4][3]; bool overflow; };
constexpr void put(Sched& s, int ks, int m, int code) { for (int e = 0; e < 3; ++e) if (s.op[ks][m][e] == 0) { s.op[ks][m][e] = code; return; } s.overflow = true; }
constexpr int weight_of(int c) { return c == 0 ? 0 : (c >= 6000 ? 1 : (c >= 4000 ? 0 : (c == 3003 ? 2 : (c == 3006 ? 4 : (((c >= 1000 && c < 3000) || c == 3001) ? 1 : 0))))); }
constexpr int ops_before(const Sched& s, int ks_end, int m_end = 0) {
  int n = 0;
  for (int ks = 0; ks < 20; ++ks) for (int m = 0; m < 4; ++m) { if (ks > ks_end || (ks == ks_end && m >= m_end)) return n; n += weight_of(s.op[ks][m][0]) + weight_of(s.op[ks][m][1]) + weight_of(s.op[ks][m][2]); }
  return n;
}
constexpr int ops_after_code(const Sched& s, int code, int ks_end, int m_end = 0) {
  int n = 0; bool seen = false;
  for (int ks = 0; ks < 20; ++ks) for (int m = 0; m < 4; ++m) {
    if (ks > ks_end || (ks == ks_end && m >= m_end)) return n;
    for (int e = 0; e < 3; ++e) { if (seen) n += weight_of(s.op[ks][m][e]); if (s.op[ks][m][e] == code) seen = true; }
  }
  return n;
}
constexpr void put_stats(Sched& s, int ks0) { for (int i = 0; i < 6; ++i) put(s, ks0 + i / 2, (i & 1) * 2, 3010 + i); }
constexpr int NPRE = 10;
constexpr int NQ0 = 30;
constexpr Sched mk_s1() {
  Sched s{};
  for (int i = NPRE; i < 20; ++i) put(s, (i - NPRE) / 2, ((i - NPRE) & 1) * 2 + 1, 6000 + i);
  for (int i = NQ0; i < 40; ++i) put(s, (i - NQ0) / 2, ((i - NQ0) & 1) * 2, 7000 + i);
  put_stats(s, 6); put(s, 5, 0, 3000); put(s, 6, 1, 3006); put(s, 11, 0, 3007); put(s, 11, 1, 3001); put(s, 16, 0, 3002); put(s, 16, 1, 3003);
  for (int i = 0; i < 7; ++i) put(s, 16 + (i + 2) / 4 + (i >= 6), i < 6 ? (i + 2) % 4 : 3, 1000 + i);
  put(s, 18, 0, 3030);
  return s;
}
constexpr Sched mk_s2() {
  Sched s{};
  put(s, 0, 0, 3020); put(s, 1, 0, 3021); put(s, 1, 2, 3022); put(s, 2, 0, 3023);
  for (int i = 0; i < 17; ++i) put(s, 1 + i, 1, 2000 + i);
  put_stats(s, 5); put(s, 9, 0, 3000); put(s, 14, 0, 3001); put(s, 18, 2, 3002);
  put(s, 18, 2, 4000); put(s, 18, 3, 4001); put(s, 19, 0, 4002); put(s, 19, 1, 4003); put(s, 19, 2, 4004); put(s, 19, 3, 4005);
  put(s, 18, 0, 3030);
  return s;
}
constexpr int GLATE0 = 12;
constexpr Sched mk_s8() {
  Sched s{};
  for (int i = NPRE; i < 20; ++i) put(s, (i - NPRE) / 2, ((i - NPRE) & 1) * 2 + 1, 6000 + i);
  for (int i = GLATE0; i < 25; ++i) put(s, 2 + (i - GLATE0) / 2 + ((i - GLATE0) >= 10 ? 0 : 0), (i - GLATE0) < 10 ? 2 : 1 + ((i - GLATE0) & 1) * 2, 2000 + 35 + i);
  put_stats(s, 5); put(s, 4, 0, 3000); put(s, 9, 0, 3001); put(s, 12, 0, 3002); put(s, 12, 0, 3003);
  for (int i = 0; i < 19; ++i) put(s, 12 + i / 3, 1 + i % 3, 1000 + i);
  put(s, 18, 0, 3030);
  return s;
}
constexpr int NBD9 = 46;
constexpr Sched mk_s9() {
  Sched s{};
  put(s, 0, 0, 3020); put(s, 0, 2, 3021); put(s, 0, 3, 3022); put(s, 1, 0, 3023);
  int f = 0;
  for (int ks = 1; ks < 20; ++ks) for (int m = 1; m < 4; ++m) { if (ks >= 12 && m == 2) continue; if (f < NBD9 && f < 3 * ks) put(s, ks, m, 2000 + f++); }
  put_stats(s, 3); put(s, 6, 0, 3000); put(s, 12, 0, 3001); put(s, 16, 0, 3002);
  for (int i = 0; i < 8; ++i) put(s, 16 + i / 2, 2, 5000 + i);
  put(s, 18, 0, 3030);
  return s;
}
constexpr Sched SS1 = mk_s1(), SS2 = mk_s2(), SS8 = mk_s8(), SS9 = mk_s9();
constexpr int count_w(const Sched& s) { int n = 0; for (int ks = 0; ks < 20; ++ks) for (int m = 0; m < 4; ++m) for (int e = 0; e < 3; ++e) if (s.op[ks][m][e] >= 2000 && s.op[ks][m][e] < 3000) ++n; return n; }
static_assert(count_w(SS9) == NBD9, "down fragments in P4(b)");
static_assert(!SS1.overflow && !SS2.overflow && !SS8.overflow && !SS9.overflow, "slot overflow");

template <int K = 0> DI void poll_issue(__amdgpu_buffer_rsrc_t rs, unsigned voff, unsigned soff) {
  asm volatile("buffer_load_dwordx2 a[%c3:%c4], %0, %1, %2 offen sc1" :: "v"(voff), "s"(rs), "s"(soff), "i"(240 + 2 * K), "i"(241 + 2 * K) : "memory");
}
template <int N, int K = 0> DI void poll_check(v2u tag, __amdgpu_buffer_rsrc_t rs, unsigned voff, unsigned soff, bool need = true) {
  v2u v;
  asm volatile("s_waitcnt vmcnt(%2)\n\tv_accvgpr_read_b32 %0, a%c3\n\tv_accvgpr_read_b32 %1, a%c4" : "=v"(v[0]), "=v"(v[1]) : "i"(N > 63 ? 63 : N), "i"(240 + 2 * K), "i"(241 + 2 * K) : "memory");
  if (need)
#pragma clang loop unroll(disable)
    for (int spins = 0; spins < (1 << 21) && !__all(v[0] == tag[0] && v[1] == tag[1]); ++spins)
      asm volatile("s_sleep 2\n\tbuffer_load_dwordx2 %0, %1, %2, %3 offen sc1\n\ts_waitcnt vmcnt(0)" : "=&v"(v) : "v"(voff), "s"(rs), "s"(soff) : "memory");
  asm volatile("" ::: "memory");
}
DI void stats_issue(__amdgpu_buffer_rsrc_t rs, unsigned voff, unsigned soff) {
  asm volatile("buffer_load_dwordx4 a[248:251], %0, %1, %2 offen sc1\n\tbuffer_load_dwordx4 a[252:255], %0, %1, %2 offen offset:16 sc1" :: "v"(voff), "s"(rs), "s"(soff) : "memory");
}
template <int N> DI void set_flag(v2u tag, __amdgpu_buffer_rsrc_t rs, unsigned soff) {
  vmcnt<N>();
  __builtin_amdgcn_raw_buffer_store_b64(tag, rs, 0, soff, 16);
  asm volatile("" ::: "memory");
}
DI v4u ldc_asm(__amdgpu_buffer_rsrc_t rs, unsigned voff, unsigned soff) {
  v4u v; asm volatile("buffer_load_dwordx4 %0, %1, %2, %3 offen sc1" : "=&v"(v) : "v"(voff), "s"(rs), "s"(soff) : "memory"); return v;
}
template <int F> DI unsigned rot5(unsigned base, int h) { constexpr int i = F / 5, n = F % 5; return base + (unsigned)(i * 5 + n) * 4096u + (h ? (unsigned)(((n + 3) % 5) - n) * 4096u : 0u); }
DI unsigned flag_off(int layer, int ph, int p, int sub) { return FLAG_OFF + (unsigned)(((layer * 5 + ph) * 16 + p) * 2 + sub) * 512; }

__global__ void __launch_bounds__(256, 1) k_main(Wts W, u16* __restrict__ out, char* __restrict__ ws) {
  const u16* x = W.x;
  extern __shared__ __attribute__((aligned(16))) char lds[];
  const int tid0 = threadIdx.x;
  const int w0 = __builtin_amdgcn_readfirstlane(tid0 >> 6);
  const int b = blockIdx.x, x8 = b & 7, rr = b >> 3;
  const int p0 = (x8 >> 1) * 4 + (rr >> 3), g0 = (x8 & 1) * 8 + (rr & 7);
  const __amdgpu_buffer_rsrc_t rsW = __builtin_amdgcn_make_buffer_rsrc((void*)ws, 0, 0x7fffffff, 0x00020000);
  const __amdgpu_buffer_rsrc_t rsX = __builtin_amdgcn_make_buffer_rsrc((void*)x, 0, S_ * D_ * 2, 0x00020000);

  asm volatile("" ::: "a0", "a1", "a2", "a3", "a4", "a5", "a6", "a7", "a8", "a9", "a10", "a11", "a12", "a13", "a14", "a15", "a16", "a17", "a18", "a19", "a20", "a21", "a22", "a23", "a24", "a25", "a26", "a27", "a28", "a29", "a30", "a31", "a32", "a33", "a34", "a35", "a36", "a37", "a38", "a39", "a40", "a41", "a42", "a43", "a44", "a45", "a46", "a47", "a48", "a49", "a50", "a51", "a52", "a53", "a54", "a55", "a56", "a57", "a58", "a59", "a60", "a61", "a62", "a63", "a64", "a65", "a66", "a67", "a68", "a69", "a70", "a71", "a72", "a73", "a74", "a75", "a76", "a77", "a78", "a79", "a80", "a81", "a82", "a83", "a84", "a85", "a86", "a87", "a88", "a89", "a90", "a91", "a92", "a93", "a94", "a95", "a96", "a97", "a98", "a99", "a100", "a101", "a102", "a103", "a104", "a105", "a106", "a107", "a108", "a109", "a110", "a111", "a112", "a113", "a114", "a115", "a116", "a117", "a118", "a119", "a120", "a121", "a122", "a123", "a124", "a125", "a126", "a127", "a128", "a129", "a130", "a131", "a132", "a133", "a134", "a135", "a136", "a137", "a138", "a139", "a140", "a141", "a142", "a143", "a144", "a145", "a146", "a147", "a148", "a149", "a150", "a151", "a152", "a153", "a154", "a155", "a156", "a157", "a158", "a159", "a160", "a161", "a162", "a163", "a164", "a165", "a166", "a167", "a168", "a169", "a170", "a171", "a172", "a173", "a174", "a175", "a176", "a177", "a178", "a179", "a180", "a181", "a182", "a183", "a184", "a185", "a186", "a187", "a188", "a189", "a190", "a191", "a192", "a193", "a194", "a195", "a196", "a197", "a198", "a199", "a200", "a201", "a202", "a203", "a204", "a205", "a206", "a207", "a208", "a209", "a210", "a211", "a212", "a213", "a214", "a215", "a216", "a217", "a218", "a219", "a220", "a221", "a222", "a223", "a224", "a225", "a226", "a227", "a228", "a229", "a230", "a231", "a232", "a233", "a234", "a235", "a236", "a237", "a238", "a239", "a240", "a241", "a242", "a243", "a244", "a245", "a246", "a247", "a248", "a249", "a250", "a251", "a252", "a253", "a254", "a255");
  v2u res[2][3];
  v2u tag;
  f32x4 cq[3], cg[3];
  {
    FRESH();
    const v2u e = __builtin_amdgcn_raw_buffer_load_b64(rsW, 0, EPOCH_OFF, 16);
    v4u va[8], vb[8];
    prep_load(b, W, va, tid, w);
    prep_load(256 + b, W, vb, tid, w);
    asm volatile("" ::: "memory");
    prep_store(b, va, rsW, lds + LDS_R1, tid, w);
    {
      const unsigned e0 = __builtin_amdgcn_readfirstlane(e[0]), e1 = __builtin_amdgcn_readfirstlane(e[1]);
      tag = v2u{e0 + 1u, e1 + (e0 == 0xffffffffu ? 1u : 0u)};
    }
    vmcnt<0>();
    BAR();
    if (w == 0) __builtin_amdgcn_raw_buffer_store_b64(tag, rsW, 0, RF_OFF + b * 8, 16);
    prep_store(256 + b, vb, rsW, lds + LDS_R1, tid, w);
#pragma unroll
    for (int s = 0; s < 2; ++s)
#pragma unroll
      for (int i = 0; i < 3; ++i)
        res[s][i] = __builtin_amdgcn_raw_buffer_load_b64(rsX, (unsigned)(r * 1280 + gq * 8),
                                                         (unsigned)((128 * p + 64 * s + 32 * rh + 16 * (w & 1)) * 1280 + 160 * c + 32 * ((i + 3 * (w >> 1)) % 5)), 0);
    poll_issue(rsW, lane * 8, RF_OFF + w * 512);
    poll_check<0>(tag, rsW, lane * 8, RF_OFF + w * 512);
    BAR();
    if (w == 0) __builtin_amdgcn_raw_buffer_store_b64(tag, rsW, 0, RF_OFF + 2048 + b * 8, 16);
    if (b == 0 && w == 1) __builtin_amdgcn_raw_buffer_store_b64(tag, rsW, 0, EPOCH_OFF, 16);
    unsigned wb = WQ_OFF + (g * 160 + w) * 1024; OPAQUE(wb);
    stats_issue(rsW, tid * 32, STX_OFF + p * 16384);
    unsigned ab = X0_OFF + p * 163840 + w * 1024, lb = LDS_R0 + w * 1024; OPAQUE(ab); OPAQUE(lb);
    sfor<NPRE>([&](auto I) { sfor<3>([&](auto K) { ldw<3 * I.value + K.value>(rsW, l16, wb + (3 * I.value + K.value) * 4096); }); dma2<I.value * 4096>(rsW, l16, ab + I.value * 4096, lb); });
  }

  float sm_, sq_;
  auto stats_stage = [&](auto STc, int sub, int tid) {
    constexpr int ST = STc.value;
    if constexpr (ST == 0) {
      float t[8];
      asm volatile("v_accvgpr_read_b32 %0, a248\n\tv_accvgpr_read_b32 %1, a249\n\tv_accvgpr_read_b32 %2, a250\n\tv_accvgpr_read_b32 %3, a251\n\t"
                   "v_accvgpr_read_b32 %4, a252\n\tv_accvgpr_read_b32 %5, a253\n\tv_accvgpr_read_b32 %6, a254\n\tv_accvgpr_read_b32 %7, a255"
                   : "=v"(t[0]), "=v"(t[1]), "=v"(t[2]), "=v"(t[3]), "=v"(t[4]), "=v"(t[5]), "=v"(t[6]), "=v"(t[7]));
      sm_ = (t[0] + t[2]) + (t[4] + t[6]); sq_ = (t[1] + t[3]) + (t[5] + t[7]);
    } else if constexpr (ST == 1) { sm_ += dpp_xor1(sm_); sq_ += dpp_xor1(sq_); }
    else if constexpr (ST == 2) { sm_ += dpp_xor2(sm_); sq_ += dpp_xor2(sq_); }
    else if constexpr (ST == 3) { sm_ *= (1.f / 640.f); sq_ = fmaxf(sq_ * (1.f / 640.f) - sm_ * sm_, 0.f); sq_ = rbf(sq_); }
    else if constexpr (ST == 4) { sq_ = rbf(__builtin_amdgcn_sqrtf(rbf(sq_ + 1e-5f))); }
    else { *(f32x2*)(lds + LDS_ST + (sub * 64 + (tid >> 2)) * 8) = f32x2{rbf(sm_), __builtin_amdgcn_rcpf(sq_)}; }
  };
  auto ring_gemm = [&](auto NJc, auto SUBc, auto NAFTERc, const Sched& dummy, auto SCH, f32x4 (&acc)[3][4], f32x4 (&c4)[3], bool do_cs, unsigned l16, auto&& bg) {
    constexpr int NJ = NJc.value, SUB = SUBc.value, NAFTER = NAFTERc.value;
    constexpr const Sched& S = SCH.get();
    unsigned rb0 = (SUB == 0 ? LDS_R0 : LDS_R1) + l16, rb1 = rb0 + 65536, rbx = LDS_R1X + l16;
    asm volatile("" : "+v"(rb0), "+v"(rb1), "+v"(rbx));
    auto rd = [&](int ks, int m) -> v4u {
      if (SUB == 1 && ks == 19) return *(const LAS v4u*)(rbx + m * 1024);
      return ks < 16 ? *(const LAS v4u*)(rb0 + ks * 4096 + m * 1024) : *(const LAS v4u*)(rb1 + (ks - 16) * 4096 + m * 1024);
    };
    auto gend = [](int k) constexpr -> int {
      if (SUB == 0) return k == 0 ? 1 : (k == 2 ? 3 : ((k % 4 == 0) ? k + 3 : -1));
      return k == 0 ? 9 : (k == 10 ? 19 : -1);
    };
    v4u xa[3][4];
    v4u ones = {0x3f803f80u, 0x3f803f80u, 0x3f803f80u, 0x3f803f80u};
    asm volatile("" : "+v"(ones));
    vmcnt<(SUB == 0 ? NPRE : 20) - 1 - gend(0) + NAFTER>();
    BAR();
#pragma unroll
    for (int k = 0; k < 2; ++k)
#pragma unroll
      for (int m = 0; m < 4; ++m) xa[k][m] = rd(k, m);
    SBAR();
    sfor<20>([&](auto KS) {
      constexpr int ks = KS.value;
      if constexpr (ks + 2 < 20 && gend(ks + 2) >= 0) {
        constexpr int e = gend(ks + 2);
        constexpr int cd = (SUB == 1 || e < NPRE) ? (SUB == 0 ? NPRE : 20) - 1 - e + NAFTER + ops_before(S, ks) : ops_after_code(S, 6000 + e, ks);
        constexpr int wneed = ks == 14 ? 39 : 2 * (ks + 3) + 1;
        constexpr int cw = (SUB == 0 && NJ == 2 && wneed >= NQ0) ? ops_after_code(S, 7000 + wneed, ks) : 63;
        vmcnt<(cd < cw ? cd : cw)>(); asm volatile("s_barrier" ::: "memory");
      }
      if constexpr (ks == 18) BAR();
      if constexpr (SUB == 0 && ks % 5 == 4) {
        if (do_cs) sfor<5>([&](auto K) { sfor<NJ>([&](auto J) {
          constexpr int k2 = ks - 4 + K.value;
          if constexpr (k2 == 0) mfma_w0<J.value>(c4[J.value], ones); else mfma_w<k2 * NJ + J.value>(c4[J.value], ones); }); });
        SBAR();
      }
      sfor<4>([&](auto M) {
        constexpr int m = M.value;
        sfor<NJ>([&](auto J) {
          constexpr int j = J.value;
          if constexpr (ks == 0) mfma_w0<j>(acc[j][m], xa[0][m]); else mfma_w<ks * NJ + j>(acc[j][m], xa[ks % 3][m]);
          if constexpr (j == 0 && ks < 18) xa[(ks + 2) % 3][m] = rd(ks + 2, m);
          if constexpr (j == 1 && S.op[ks][m][0] != 0) bg(IC<S.op[ks][m][0]>{}, KS, M);
          if constexpr (j == NJ - 1 && S.op[ks][m][1] != 0) bg(IC<S.op[ks][m][1]>{}, KS, M);
          if constexpr (j == NJ - 1 && S.op[ks][m][2] != 0) bg(IC<S.op[ks][m][2]>{}, KS, M);
          SBAR();
        });
      });
    });
    MFMA_DRAIN();
  };

#pragma unroll 1
  for (int layer = 0; layer < 2; ++layer) {
    const unsigned LB = ACT_OFF + layer * LAYER_BYTES;
    {
      FRESH();
      STAMP(0);
      f32x4 (&c4)[3] = cq;
      unsigned abase = (layer == 0 ? X0_OFF : ACT_OFF + L_X3) + p * 163840 + 81920 + w * 1024;
      const unsigned stb = (layer == 0 ? STX_OFF : ACT_OFF + L_S3) + p * 16384 + 8192;
      unsigned lb1 = LDS_R1 + w * 1024, wqb = WQ_OFF + (g * 160 + w) * 1024, wgb = WG_OFF + (g * 240 + w) * 1024;
      OPAQUE(abase); OPAQUE(lb1); OPAQUE(wqb); OPAQUE(wgb);
      f32x4 acc[3][4];
      f32x2 st[4]; v4u sv[3];
      auto epilogue1 = [&](auto SUBc, auto&& mid) {
        constexpr int SUB = SUBc.value;
        constexpr int KVO = (SUB == 0 ? LDS_KVA : LDS_KVB) + 8 * TP;
        if (tid < 8) *(v4u*)(lds + KVO + (tid >> 1) * TP + 2560 + (tid & 1) * 16) = v4u{(tid & 1) ? 0u : 0x00003f80u, 0, 0, 0};
        mid(IC<0>{});
        SBAR();
        sfor<2>([&](auto J) {
          constexpr int j = J.value;
          const int pc0 = 16 * (2 * w + j) + 4 * gq;
          const int a0 = pc0 < 40 ? LDS_Q + (SUB * 64 + r) * 80 + pc0 * 2 : (pc0 < 120 ? KVO + r * 160 + (pc0 - 40) * 2 : KVO + 2688 + r * 8);
          const int ts = pc0 < 40 ? 1280 : TP;
          const f32x2 ca = {c4[j][0], c4[j][1]}, cb = {c4[j][2], c4[j][3]};
          sfor<4>([&](auto M) {
            constexpr int m = M.value;
            const f32x2 nm = {-st[m][0], -st[m][0]}, rs2 = {st[m][1], st[m][1]};
            f32x2 ya = __builtin_elementwise_fma(nm, ca, f32x2{acc[j][m][0], acc[j][m][1]}) * rs2;
            f32x2 yb = __builtin_elementwise_fma(nm, cb, f32x2{acc[j][m][2], acc[j][m][3]}) * rs2;
            *(v2u*)(lds + a0 + m * ts) = v2u{pk2(ya[0], ya[1]), pk2(yb[0], yb[1])};
          });
          sfor<4>([&](auto M) { mid(IC<1 + j * 4 + M.value>{}); });
          SBAR();
        });
      };
      struct G1 { static constexpr const Sched& get() { return SS1; } };
      ring_gemm(IC<2>{}, IC<0>{}, IC<0>{}, SS1, G1{}, acc, cq, layer == 0, l16, [&](auto OP, auto KS, auto M) {
        constexpr int op = OP.value;
        if constexpr (op >= 7000) ldw<op - 7000>(rsW, l16, wqb + (op - 7000) * 4096);
        else if constexpr (op >= 6000) dma2<(op - 6000) * 4096 + LDS_R0 - LDS_R1>(rsW, l16, abase - 81920 + (op - 6000) * 4096, lb1);
        else if constexpr (op >= 1000 && op < 2000) dma2<(op - 1000) * 4096>(rsW, l16, abase + (op - 1000) * 4096, lb1);
        else if constexpr (op == 3000) { if (layer > 0) set_flag<ops_before(SS1, KS.value, M.value)>(tag, rsW, flag_off(0, PH_X3, p, 1) + (g * 4 + w) * 8); }
        else if constexpr (op == 3001) poll_issue(rsW, lane * 8, flag_off(0, PH_X3, p, 1));
        else if constexpr (op == 3002) poll_check<ops_after_code(SS1, 3001, KS.value, M.value)>(tag, rsW, lane * 8, flag_off(0, PH_X3, p, 1), layer > 0);
        else if constexpr (op == 3006) sfor<4>([&](auto K) { poll_issue<K.value>(rsW, lane * 8, RF_OFF + 2048 + K.value * 512); });
        else if constexpr (op == 3007) sfor<4>([&](auto K) { poll_check<ops_after_code(SS1, 3006, KS.value, M.value), K.value>(tag, rsW, lane * 8, RF_OFF + 2048 + K.value * 512, layer == 0); });
        else if constexpr (op == 3003) { stats_issue(rsW, tid * 32, stb); }
        else if constexpr (op >= 3010 && op < 3016) stats_stage(IC<op - 3010>{}, 0, tid);
        else if constexpr (op == 3030) {
#pragma unroll
          for (int m = 0; m < 4; ++m) st[m] = *(const f32x2*)(lds + LDS_ST + (m * 16 + r) * 8);
        }
      });
      STAMP(1);
      epilogue1(IC<0>{}, [&](auto I) {
        constexpr int i = I.value;
        if constexpr (i == 0) {
        } else if constexpr (i <= 6) { dma2<(5 + 2 * i) * 4096>(rsW, l16, abase + (5 + 2 * i) * 4096, lb1); dma2<(6 + 2 * i) * 4096>(rsW, l16, abase + (6 + 2 * i) * 4096, lb1); }
        else if constexpr (i == 7) dma2<LDS_R1X - LDS_R1>(rsW, l16, abase + 19 * 4096, lb1);
      });
      STAMP(2);
      const unsigned kvf_v = (unsigned)((((lane >> 2) & 1) && p >= 2 ? 1024 : 0) + (g * 4 + (lane & 3)) * 8);
      const unsigned kvf_s = flag_off(layer, PH_KV, p >= 2 ? p - 2 : 0, 0);
      unsigned kb1 = (p >= 1 ? LB + L_KV + ((p - 1) * 16 + g) * 24576 : NULL_OFF) + w * 1024, kb2 = (p >= 2 ? LB + L_KV + ((p - 2) * 16 + g) * 24576 : NULL_OFF) + w * 1024, lkb = w * 1024;
      OPAQUE(kb1); OPAQUE(kb2); OPAQUE(lkb);
      struct G2 { static constexpr const Sched& get() { return SS2; } };
      ring_gemm(IC<2>{}, IC<1>{}, IC<0>{}, SS2, G2{}, acc, cq, false, l16, [&](auto OP, auto KS, auto M) {
        constexpr int op = OP.value;
        if constexpr (op >= 2000 && op < 3000) ldw<op - 2000>(rsW, l16, wgb + (op - 2000) * 4096);
        else if constexpr (op == 3000) set_flag<ops_after_code(SS2, 3023, KS.value, M.value)>(tag, rsW, flag_off(layer, PH_KV, p, 0) + (g * 4 + w) * 8);
        else if constexpr (op == 3001) poll_issue(rsW, kvf_v, kvf_s);
        else if constexpr (op == 3002) poll_check<ops_after_code(SS2, 3001, KS.value, M.value)>(tag, rsW, kvf_v, kvf_s, p > 0);
        else if constexpr (op >= 4000 && op < 4003) dma2<LDS_KVA + 4 * TP + (op - 4000) * 4096>(rsW, l16, kb1 + (op - 4000) * 4096, lkb);
        else if constexpr (op >= 4003 && op < 4006) dma2<LDS_KVA + (op - 4003) * 4096>(rsW, l16, kb2 + (op - 4003) * 4096, lkb);
        else if constexpr (op >= 3010 && op < 3016) stats_stage(IC<op - 3010>{}, 1, tid);
        else if constexpr (op == 3020) {
#pragma unroll
          for (int i = 0; i < 3; ++i) sv[i] = *(const v4u*)(lds + LDS_KVA + 8 * TP + (tid + 256 * i) * 16);
        } else if constexpr (op > 3020 && op <= 3023) { stc(sv[op - 3021], rsW, (unsigned)(tid + 256 * (op - 3021)) * 16, LB + L_KV + (p * 16 + g) * 24576); asm volatile("" ::: "memory"); }
        else if constexpr (op == 3030) {
#pragma unroll
          for (int m = 0; m < 4; ++m) st[m] = *(const f32x2*)(lds + LDS_ST + (64 + m * 16 + r) * 8);
        }
      });
      STAMP(3);
      epilogue1(IC<1>{}, [&](auto) {});
      STAMP(4);
      vmcnt<0>();
      BAR();
    }
    v4u af[24];
    {
      FRESH();
      unsigned wob = WO_OFF + (c * 100 + w) * 1024, wgb = WG_OFF + (g * 240 + w) * 1024; OPAQUE(wob); OPAQUE(wgb);
      const unsigned kvf_v = (unsigned)((((lane >> 2) & 1) && p >= 2 ? 1024 : 0) + (g * 4 + (lane & 3)) * 8);
      const unsigned kvf_s = flag_off(layer, PH_KV, p >= 2 ? p - 2 : 0, 1);
      unsigned kb1 = (p >= 1 ? LB + L_KV + ((p - 1) * 16 + g) * 24576 + 12288 : NULL_OFF) + w * 1024, kb2 = (p >= 2 ? LB + L_KV + ((p - 2) * 16 + g) * 24576 + 12288 : NULL_OFF) + w * 1024, lkb = w * 1024;
      OPAQUE(kb1); OPAQUE(kb2); OPAQUE(lkb);
      const int hh = w & 1, jb = (w >> 1) * 2;
      const float SC = 0.22360679774997896f * 1.4426950408889634f;
      const int q4 = (lane & 15) >> 2, p4 = lane & 3;
      const unsigned koff = gq < 3 ? r * 160 + hh * 80 + 16 * gq : 2576;
      const unsigned voff0 = (4 * gq + q4) * 160 + hh * 80 + 40 + 8 * p4;
      const unsigned voff1 = p4 == 0 ? (4 * gq + q4) * 160 + hh * 80 + 40 + 32 : (p4 == 1 ? 2560 : 2568);
      bool up[4];
#pragma unroll
      for (int e = 0; e < 4; ++e) up[e] = (4 * gq + e) > r;
      const unsigned mA0 = (up[0] ? 0xffffu : 0u) | (up[1] ? 0xffff0000u : 0u), mA1 = (up[2] ? 0xffffu : 0u) | (up[3] ? 0xffff0000u : 0u);
      f32x4 o0[4], o1[4]; float mx[4];
      v4u kf[10]; v4u qf[2]; v2u qlo[2], qhi[2];
      auto dbase = [&](int D) -> const char* { return lds + (D < 2 ? LDS_KVA : LDS_KVB) + ((D == 0 || D == 3) ? jb : (D == 1 ? 4 : 0)) * TP; };
      auto qrow_of = [&](int D, int u) -> char* { return lds + LDS_Q + (16 * ((D & 1) * 4 + jb + u) + r) * 80 + hh * 40; };
      auto read_q = [&](int D) {
#pragma unroll
        for (int u = 0; u < 2; ++u) {
          qlo[u] = *(const v2u*)(qrow_of(D, u) + (gq < 2 ? 16 * gq : 32));
          qhi[u] = *(const v2u*)(qrow_of(D, u) + (gq < 2 ? 16 * gq + 8 : 32));
        }
      };
      auto make_q = [&]() {
#pragma unroll
        for (int u = 0; u < 2; ++u) qf[u] = v4u{gq < 3 ? qlo[u][0] : 0x3f80u, gq < 3 ? qlo[u][1] : 0u, gq < 2 ? qhi[u][0] : 0u, gq < 2 ? qhi[u][1] : 0u};
        asm volatile("" : "+v"(qf[0]), "+v"(qf[1]));
      };
      auto read_k = [&](int D, int t) { kf[t] = *(const v4u*)(dbase(D) + t * TP + koff); };
      auto load_kq = [&](auto Dc) {
        constexpr int D = Dc.value;
        read_q(D);
#pragma unroll
        for (int t = 0; t < ((D == 0 || D == 3) ? 10 : 8); ++t) read_k(D, t);
        make_q();
        SBAR();
      };
      auto dchunk = [&](auto Dc, auto PIPEc, auto&& bg) {
        constexpr int D = Dc.value, UB = (D & 1) * 2; constexpr bool MASKED = D == 0 || D == 3, FIRST = D < 2, PIPE = PIPEc.value != 0;
        constexpr int NT = MASKED ? 9 : 8, NP = MASKED ? 5 : 4, NKN = (D + 1 == 3) ? 10 : 8;
        f32x4 s[2][9]; v4u vq0[5], vq1[5];
        const char* kvb = dbase(D);
        auto read_v = [&](int pr, int hf) {
          const v2u a = __builtin_bit_cast(v2u, __builtin_amdgcn_ds_read_tr16_b64_v4i16((LAS s16x4*)(kvb + 2 * pr * TP + (hf ? voff1 : voff0))));
          const v2u bq = __builtin_bit_cast(v2u, __builtin_amdgcn_ds_read_tr16_b64_v4i16((LAS s16x4*)(kvb + (2 * pr + 1) * TP + (hf ? voff1 : voff0))));
          (hf ? vq1 : vq0)[pr] = v4u{a[0], a[1], bq[0], bq[1]};
        };
        float ma[2], mb[2];
        auto tord = [](int i) constexpr -> int { return MASKED ? (i == 0 ? 0 : (i == 1 ? 8 : i - 1)) : i; };
        auto maxstep = [&](auto Ic) {
          constexpr int i = Ic.value;
#pragma unroll
          for (int u = 0; u < 2; ++u) {
            if constexpr (MASKED && i == 0) {}
            else if constexpr (MASKED && i == 1) {
#pragma unroll
              for (int e = 0; e < 4; ++e) s[u][0][e] = up[e] ? s[u][0][e] : s[u][8][e];
              ma[u] = max3(s[u][0][0], s[u][0][1], s[u][0][1]); mb[u] = max3(s[u][0][2], s[u][0][3], s[u][0][3]);
            } else if constexpr (i == 0) { ma[u] = max3(s[u][0][0], s[u][0][1], s[u][0][1]); mb[u] = max3(s[u][0][2], s[u][0][3], s[u][0][3]); }
            else { constexpr int t = tord(i); ma[u] = max3(ma[u], s[u][t][0], s[u][t][1]); mb[u] = max3(mb[u], s[u][t][2], s[u][t][3]); }
          }
        };
        asm volatile("s_nop 1" ::: "memory");
        sfor<NT>([&](auto I) {
          constexpr int i = I.value, t = tord(i);
          mfma_n0(s[0][t], kf[t], qf[0]);
          if constexpr (i < 2 * NP) read_v(i >> 1, i & 1);
          SBAR();
          mfma_n0(s[1][t], kf[MASKED ? t + 1 : t], qf[1]);
          if constexpr (i >= 2) maxstep(IC<i - 2>{});
          SBAR();
        });
        if constexpr (MASKED) read_v(4, 1);
        asm volatile("s_nop 7" ::: "memory");
        SBAR();
        maxstep(IC<NT - 2>{});
        asm volatile("s_nop 3" ::: "memory");
        SBAR();
        maxstep(IC<NT - 1>{});
        SBAR();
        bg(IC<0>{});
        if constexpr (PIPE) read_q(D + 1);
        float mc[2];
#pragma unroll
        for (int u = 0; u < 2; ++u) {
          const float m = max1632(FIRST ? max3(ma[u], mb[u], mb[u]) : max3(ma[u], mb[u], mx[UB + u]));
          mc[u] = -m * SC;
          if constexpr (!FIRST) {
            const float al = __builtin_amdgcn_exp2f(fmaf(mx[UB + u], SC, mc[u]));
            o0[UB + u] *= al; o1[UB + u] *= al;
          }
          mx[UB + u] = m;
        }
        const f32x2 sc2 = {SC, SC};
#pragma unroll
        for (int t = 0; t < 8; ++t)
#pragma unroll
          for (int u = 0; u < 2; ++u) {
            const f32x2 za = __builtin_elementwise_fma(f32x2{s[u][t][0], s[u][t][1]}, sc2, f32x2{mc[u], mc[u]});
            const f32x2 zb = __builtin_elementwise_fma(f32x2{s[u][t][2], s[u][t][3]}, sc2, f32x2{mc[u], mc[u]});
            s[u][t] = f32x4{za[0], za[1], zb[0], zb[1]};
          }
        SBAR();
        unsigned pp[2][8][2], pe[2][4];
        auto pv = [&](auto PRc, auto Uc, auto Hc, auto F0c) {
          constexpr int pr = PRc.value, u = Uc.value, hsel = Hc.value;
          v4u pf;
          if constexpr (!MASKED) pf = v4u{pp[u][2 * pr][0], pp[u][2 * pr][1], pp[u][2 * pr + 1][0], pp[u][2 * pr + 1][1]};
          else if constexpr (u == 0) {
            if constexpr (pr == 0) pf = v4u{pe[0][0], pe[0][1], pp[0][1][0], pp[0][1][1]};
            else if constexpr (pr == 4) pf = v4u{pe[0][2], pe[0][3], 0u, 0u};
            else pf = v4u{pp[0][2 * pr][0], pp[0][2 * pr][1], pp[0][2 * pr + 1][0], pp[0][2 * pr + 1][1]};
          } else {
            if constexpr (pr == 0) pf = v4u{0u, 0u, pe[1][0], pe[1][1]};
            else if constexpr (pr == 4) pf = v4u{pp[1][7][0], pp[1][7][1], pe[1][2], pe[1][3]};
            else pf = v4u{pp[1][2 * pr - 1][0], pp[1][2 * pr - 1][1], pp[1][2 * pr][0], pp[1][2 * pr][1]};
          }
          f32x4& o = hsel == 0 ? o0[UB + u] : o1[UB + u];
          if constexpr (FIRST && F0c.value != 0) mfma_v0(o, hsel == 0 ? vq0[pr] : vq1[pr], pf); else mfma_v(o, hsel == 0 ? vq0[pr] : vq1[pr], pf);
        };
        auto rdy = [](int u, int t) constexpr -> int {
          if (!MASKED) return (t & 1) ? t >> 1 : -1;
          if (u == 0) return t == 0 ? 4 : ((t & 1) ? t >> 1 : -1);
          return t == 0 ? 0 : (t == 7 ? 4 : ((t & 1) == 0 ? t >> 1 : -1));
        };
        auto firstp = [](int u) constexpr -> int { return MASKED ? (u == 0 ? 4 : 0) : 0; };
        sfor<10>([&](auto T) {
          constexpr int t = T.value;
          sfor<2>([&](auto Uc) {
            constexpr int u = Uc.value;
            if constexpr (t < 8) { s[u][t][0] = __builtin_amdgcn_exp2f(s[u][t][0]); s[u][t][1] = __builtin_amdgcn_exp2f(s[u][t][1]); }
            SBAR();
            if constexpr (t >= 2 && rdy(u, t - 2) >= 0) pv(IC<rdy(u, t - 2)>{}, Uc, IC<0>{}, IC<rdy(u, t - 2) == firstp(u)>{});
            else if constexpr (PIPE && t < 8 && 2 * t + u < NKN && t < 5) read_k(D + 1, 2 * t + u);
            SBAR();
            if constexpr (t < 8) { s[u][t][2] = __builtin_amdgcn_exp2f(s[u][t][2]); s[u][t][3] = __builtin_amdgcn_exp2f(s[u][t][3]); }
            if constexpr (t >= 1 && t <= 8) {
              pp[u][t - 1][0] = pk2(s[u][t - 1][0], s[u][t - 1][1]); pp[u][t - 1][1] = pk2(s[u][t - 1][2], s[u][t - 1][3]);
              if constexpr (MASKED && t == 1) { pe[u][0] = pp[u][0][0] & mA0; pe[u][1] = pp[u][0][1] & mA1; pe[u][2] = pp[u][0][0] & ~mA0; pe[u][3] = pp[u][0][1] & ~mA1; }
            }
            SBAR();
            if constexpr (t >= 2 && rdy(u, t - 2) >= 0) {
              pv(IC<rdy(u, t - 2)>{}, Uc, IC<1>{}, IC<rdy(u, t - 2) == firstp(u)>{});
              if constexpr (PIPE && 2 * t + u < NKN && t < 5) read_k(D + 1, 2 * t + u);
            }
            SBAR();
          });
          if constexpr (t < 8) bg(IC<1 + t>{});
        });
        if constexpr (PIPE) make_q();
        SBAR();
        if constexpr (!FIRST) {
          MFMA_DRAIN();
#pragma unroll
          for (int u = 0; u < 2; ++u) {
            char* qrow = qrow_of(D, u);
            const float inv = __builtin_amdgcn_rcpf(bcast_row1(o1[UB + u][0]));
            *(v2u*)(qrow + 8 * gq) = v2u{pk2(o0[UB + u][0] * inv, o0[UB + u][1] * inv), pk2(o0[UB + u][2] * inv, o0[UB + u][3] * inv)};
            *(v2u*)(gq == 0 ? qrow + 32 : lds + LDS_KVB + 8 * TP + 2816 + lane * 2 - (lane & 3) * 2 + 0) = v2u{pk2(o1[UB + u][0] * inv, o1[UB + u][1] * inv), pk2(o1[UB + u][2] * inv, o1[UB + u][3] * inv)};
          }
        }
        SBAR();
      };
      auto nobg = [&](auto) {};
      auto store_o = [&](auto SUBc, auto&& mid) {
        constexpr int SUB = SUBc.value;
        BAR();
        v4u v[2];
        const int mt = (tid >> 4) & 3;
        v[0] = *(const v4u*)(lds + LDS_Q + (SUB * 64 + 16 * mt + r) * 80 + 16 * w);
        v[1] = *(const v4u*)(lds + LDS_Q + (SUB * 64 + 16 * mt + r) * 80 + 64);
        mid(IC<0>{});
        SBAR();
        const unsigned vo = (unsigned)((mt >> 1) * 40960 + (mt & 1) * 1024 + r * 16), ob = LB + L_O + p * 163840 + SUB * 81920;
        { const int c8 = 5 * g + w; stc(v[0], rsW, vo, ob + (c8 >> 2) * 2048 + (c8 & 3) * 256); }
        if (w == 0) { const int c8 = 5 * g + 4; stc(v[1], rsW, vo, ob + (c8 >> 2) * 2048 + (c8 & 3) * 256); }
        asm volatile("" ::: "memory");
      };
      {
        v4u sv[3];
#pragma unroll
        for (int i = 0; i < 3; ++i) sv[i] = *(const v4u*)(lds + LDS_KVB + 8 * TP + (tid + 256 * i) * 16);
        load_kq(IC<0>{});
#pragma unroll
        for (int i = 0; i < 3; ++i) stc(sv[i], rsW, (unsigned)(tid + 256 * i) * 16, LB + L_KV + (p * 16 + g) * 24576 + 12288);
        asm volatile("" ::: "memory");
      }
      dchunk(IC<0>{}, IC<1>{}, [&](auto I) {
        if constexpr (I.value == 2) set_flag<0>(tag, rsW, flag_off(layer, PH_KV, p, 1) + (g * 4 + w) * 8);
        if constexpr (I.value >= 3) sfor<2>([&](auto K) { constexpr int f = (I.value - 3) * 2 + K.value; ldw<40 + f>(rsW, l16, rot5<f>(wob, w >> 1)); });
        if constexpr (I.value >= 3) ldw<17 + I.value - 3>(rsW, l16, wgb + (17 + I.value - 3) * 4096);
      });
      dchunk(IC<1>{}, IC<0>{}, [&](auto I) {
        if constexpr (I.value == 0) poll_issue(rsW, kvf_v, kvf_s);
        if constexpr (I.value == 4) {
          poll_check<0>(tag, rsW, kvf_v, kvf_s, p > 0);
          sfor<3>([&](auto K) { dma2<LDS_KVB + 4 * TP + K.value * 4096>(rsW, l16, kb1 + K.value * 4096, lkb); });
        }
        if constexpr (I.value == 5) sfor<3>([&](auto K) { dma2<LDS_KVB + K.value * 4096>(rsW, l16, kb2 + K.value * 4096, lkb); });
      });
      STAMP(5);
      vmcnt<0>();
      BAR();
      STAMP(6);
      load_kq(IC<2>{});
      dchunk(IC<2>{}, IC<1>{}, [&](auto I) {
        if constexpr (I.value >= 1 && I.value <= 4) sfor<2>([&](auto K) { constexpr int f = 12 + (I.value - 1) * 2 + K.value; ldw<40 + f>(rsW, l16, rot5<f>(wob, w >> 1)); });
        if constexpr (I.value >= 4) { constexpr int f = 20 + I.value - 4; ldw<35 + f - 20>(rsW, l16, rot5<f>(wob, w >> 1)); }
      });
      store_o(IC<0>{}, nobg);
      STAMP(7);
      dchunk(IC<3>{}, IC<0>{}, [&](auto I) {
        if constexpr (I.value == 1) set_flag<0>(tag, rsW, flag_off(layer, PH_O, p, 0) + (g * 4 + w) * 8);
        if constexpr (I.value >= 2 && I.value <= 5) sfor<3>([&](auto K) { constexpr int f = 23 + (I.value - 2) * 3 + K.value; ldw<f>(rsW, l16, wgb + f * 4096); });
        if constexpr (I.value == 8) poll_issue(rsW, lane * 8, flag_off(layer, PH_O, p, 0));
      });
      unsigned ob = LB + L_O + p * 163840 + rh * 40960 + w * 2048; OPAQUE(ob);
      store_o(IC<1>{}, [&](auto) {
        poll_check<0>(tag, rsW, lane * 8, flag_off(layer, PH_O, p, 0));
#pragma unroll
        for (int i = 0; i < 5; ++i)
#pragma unroll
          for (int m = 0; m < 2; ++m) af[i * 2 + m] = ldc(rsW, l16 + (m ^ (w & 1)) * 1024, ob + i * 8192);
        asm volatile("" ::: "memory");
      });
      STAMP(8);
    }
    auto reduce_epilogue = [&](auto SUBc, f32x4 (&acc)[5][2], unsigned xoff, unsigned soff, bool last, int tid, int w, int p, int g, auto&& mid) {
      constexpr int SUB = SUBc.value;
      const int lane = tid & 63, r = tid & 15, gq = (tid >> 4) & 3, rh = g & 1, c = g >> 1, mo = w & 1, h = w >> 1;
      char* rb = lds + LDS_RED + lane * 16;
#pragma unroll
      for (int n = 0; n < 5; ++n) {
        const int np = (n + 3 * h) % 5;
        *(f32x4*)(rb + (((mo ^ 1) * 5 + np) * 4 + w) * 1024) = acc[n][1];
        if (n >= 2) *(f32x4*)(rb + ((mo * 5 + np) * 4 + w) * 1024) = acc[n][0];
      }
      mid(IC<0>{});
      BAR();
      f32x4 rd[3][3];
#pragma unroll
      for (int n = 0; n < 3; ++n)
#pragma unroll
        for (int q = 1; q < 4; ++q) rd[n][q - 1] = *(const f32x4*)(rb + ((mo * 5 + (n + 3 * h) % 5) * 4 + ((w + q) & 3)) * 1024);
      mid(IC<1>{});
      SBAR();
      float sm = 0.f, sq = 0.f;
#pragma unroll
      for (int n = 0; n < 3; ++n) {
        const f32x4 sv = (acc[n][0] + rd[n][0]) + (rd[n][1] + rd[n][2]);
        const unsigned a0 = pk2(sv[0], sv[1]), a1 = pk2(sv[2], sv[3]);
        const v2u o = {pk2(bf2f(res[SUB][n][0] & 0xffffu) + bf2f(a0 & 0xffffu), bf2f(res[SUB][n][0] >> 16) + bf2f(a0 >> 16)),
                       pk2(bf2f(res[SUB][n][1] & 0xffffu) + bf2f(a1 & 0xffffu), bf2f(res[SUB][n][1] >> 16) + bf2f(a1 >> 16))};
        res[SUB][n] = o;
        const unsigned one = (n < 2 || h == 0) ? 0x3f803f80u : 0u, msk = (n < 2 || h == 0) ? 0xffffffffu : 0u;
        sm = dot2(o[0], one, sm); sq = dot2(o[0] & msk, o[0], sq); sm = dot2(o[1], one, sm); sq = dot2(o[1] & msk, o[1], sq);
      }
      sm = sum32(sum16(sm)); sq = sum32(sum16(sq));
      SBAR();
      mid(IC<2>{});
      SBAR();
#pragma unroll
      for (int n = 0; n < 3; ++n) {
        if (n < 2 || h == 0) {
          const int np = (n + 3 * h) % 5;
          if (!last) {
            const int c8 = 10 * c + 2 * np;
            stc2(res[SUB][n], rsW, (unsigned)((gq >> 1) * 256 + r * 16 + (gq & 1) * 8), xoff + SUB * 81920 + (2 * rh + mo) * 1024 + (c8 >> 2) * 4096 + (c8 & 3) * 256);
          } else {
            *(v2u*)(out + (size_t)(128 * p + 64 * SUB + 32 * rh + 16 * mo + r) * 640 + 80 * c + 16 * np + 4 * gq) = res[SUB][n];
          }
        }
      }
      if (!last) {
        if (lane < 16) stc2(v2u{__builtin_bit_cast(unsigned, sm), __builtin_bit_cast(unsigned, sq)}, rsW, (unsigned)(r * 128),
                            soff + (64 * SUB + 32 * rh + 16 * mo) * 128 + (c * 2 + h) * 8);
      }
      asm volatile("" ::: "memory");
    };
    {
      FRESH();
      unsigned ob = LB + L_O + p * 163840 + rh * 40960 + w * 2048; OPAQUE(ob);
      unsigned wgb = WG_OFF + (g * 240 + w) * 1024; OPAQUE(wgb);
      STAMP(9);
      f32x4 acc[5][2];
      auto p3_mma = [&](auto OFFc, auto&& mid) {
        constexpr int OFF = OFFc.value;
        sfor<5>([&](auto I) {
          mid(I);
          sfor<2>([&](auto M) { sfor<5>([&](auto N) {
            constexpr int i = I.value, m = M.value, n = N.value;
            if constexpr (i == 0) mfma_w0<40 + n>(acc[n][m], af[OFF + m]); else mfma_w<(i < 4 ? 40 + i * 5 + n : 35 + n)>(acc[n][m], af[OFF + i * 2 + m]);
          }); });
        });
        MFMA_DRAIN();
      };
      p3_mma(IC<0>{}, [&](auto I) { if constexpr (I.value == 2) set_flag<0>(tag, rsW, flag_off(layer, PH_O, p, 1) + (g * 4 + w) * 8); });
      reduce_epilogue(IC<0>{}, acc, LB + L_X2 + p * 163840, LB + L_S2 + p * 16384, false, tid, w, p, g, [&](auto I) {
        if constexpr (I.value == 0) poll_issue(rsW, lane * 8, flag_off(layer, PH_O, p, 1));
        if constexpr (I.value == 2) {
          poll_check<0>(tag, rsW, lane * 8, flag_off(layer, PH_O, p, 1));
#pragma unroll
          for (int i = 0; i < 5; ++i)
#pragma unroll
            for (int m = 0; m < 2; ++m) af[10 + i * 2 + m] = ldc(rsW, l16 + (m ^ (w & 1)) * 1024, ob + 81920 + i * 8192);
          asm volatile("" ::: "memory");
        }
      });
      STAMP(10);
      BAR();
      p3_mma(IC<10>{}, [&](auto I) { if constexpr (I.value == 3) set_flag<0>(tag, rsW, flag_off(layer, PH_X2, p, 0) + (g * 4 + w) * 8); });
      unsigned ab = LB + L_X2 + p * 163840 + w * 1024, lb = LDS_R0 + w * 1024; OPAQUE(ab); OPAQUE(lb);
      reduce_epilogue(IC<1>{}, acc, LB + L_X2 + p * 163840, LB + L_S2 + p * 16384, false, tid, w, p, g, [&](auto I) {
        if constexpr (I.value == 0) sfor<GLATE0>([&](auto K) { ldw<35 + K.value>(rsW, l16, wgb + (35 + K.value) * 4096); });
        if constexpr (I.value == 1) poll_issue(rsW, lane * 8, flag_off(layer, PH_X2, p, 0));
        if constexpr (I.value == 2) {
          poll_check<0>(tag, rsW, lane * 8, flag_off(layer, PH_X2, p, 0));
          STAMP(11);
          stats_issue(rsW, tid * 32, LB + L_S2 + p * 16384);
          sfor<NPRE>([&](auto K) { dma2<K.value * 4096>(rsW, l16, ab + K.value * 4096, lb); });
        }
      });
      STAMP(12);
    }
    {
      FRESH();
      f32x4 (&c4)[3] = cg;
      unsigned abase = LB + L_X2 + p * 163840 + 81920 + w * 1024, lb1 = LDS_R1 + w * 1024;
      unsigned wgb = WG_OFF + (g * 240 + w) * 1024, wdb = WD_OFF + (c * 240 + w) * 1024;
      unsigned hb = LB + L_H + p * 393216 + rh * 98304 + w * 2048;
      OPAQUE(abase); OPAQUE(lb1); OPAQUE(wgb); OPAQUE(wdb); OPAQUE(hb);
      f32x4 acc[3][4];
      f32x2 st[4]; v4u sv[3];
      const int mt = (tid >> 4) & 3;
      const unsigned lm0 = l16 + (w & 1) * 1024, lm1 = l16 + ((w & 1) ^ 1) * 1024;
      auto epilogue4 = [&](auto SUBc, auto&& mid) {
        constexpr int SUB = SUBc.value;
        mid(IC<0>{});
        SBAR();
        sfor<3>([&](auto J) {
          constexpr int j = J.value;
          const f32x2 ca = {c4[j][0], c4[j][1]}, cb = {c4[j][2], c4[j][3]};
          sfor<4>([&](auto M) {
            constexpr int m = M.value;
            const f32x2 nm = {-st[m][0], -st[m][0]}, rs2 = {st[m][1], st[m][1]};
            const float rl = st[m][1] * -1.4426950408889634f;
            f32x2 ge = __builtin_elementwise_fma(nm, ca, f32x2{acc[j][m][0], acc[j][m][1]}) * f32x2{rl, rl};
            f32x2 uu = __builtin_elementwise_fma(nm, cb, f32x2{acc[j][m][2], acc[j][m][3]}) * rs2;
            f32x2 dn = f32x2{__builtin_amdgcn_exp2f(ge[0]), __builtin_amdgcn_exp2f(ge[1])} + f32x2{1.f, 1.f};
            f32x2 hv = uu * f32x2{__builtin_amdgcn_rcpf(dn[0]), __builtin_amdgcn_rcpf(dn[1])};
            *(unsigned*)(lds + LDS_H + SUB * 16384 + (m * 16 + r) * HP + (24 * w + 8 * j + 2 * gq) * 2) = pk2(hv[0], hv[1]);
          });
          sfor<4>([&](auto M) { mid(IC<1 + j * 4 + M.value>{}); });
          SBAR();
        });
      };
      struct G8 { static constexpr const Sched& get() { return SS8; } };
      ring_gemm(IC<3>{}, IC<0>{}, IC<0>{}, SS8, G8{}, acc, cg, layer == 0, l16, [&](auto OP, auto KS, auto M) {
        constexpr int op = OP.value;
        if constexpr (op >= 6000) dma2<(op - 6000) * 4096 + LDS_R0 - LDS_R1>(rsW, l16, abase - 81920 + (op - 6000) * 4096, lb1);
        else if constexpr (op >= 2000 && op < 3000) ldw<op - 2000>(rsW, l16, wgb + (op - 2000) * 4096);
        else if constexpr (op >= 1000 && op < 2000) dma2<(op - 1000) * 4096>(rsW, l16, abase + (op - 1000) * 4096, lb1);
        else if constexpr (op == 3000) set_flag<ops_before(SS8, KS.value, M.value)>(tag, rsW, flag_off(layer, PH_X2, p, 1) + (g * 4 + w) * 8);
        else if constexpr (op == 3001) poll_issue(rsW, lane * 8, flag_off(layer, PH_X2, p, 1));
        else if constexpr (op == 3002) poll_check<ops_after_code(SS8, 3001, KS.value, M.value)>(tag, rsW, lane * 8, flag_off(layer, PH_X2, p, 1));
        else if constexpr (op == 3003) { stats_issue(rsW, tid * 32, LB + L_S2 + p * 16384 + 8192); }
        else if constexpr (op >= 3010 && op < 3016) stats_stage(IC<op - 3010>{}, 0, tid);
        else if constexpr (op == 3030) {
#pragma unroll
          for (int m = 0; m < 4; ++m) st[m] = *(const f32x2*)(lds + LDS_ST + (m * 16 + r) * 8);
        }
      });
      STAMP(13);
      epilogue4(IC<0>{}, [&](auto I) { if constexpr (I.value == 0) dma2<LDS_R1X - LDS_R1>(rsW, l16, abase + 19 * 4096, lb1); });
      STAMP(14);
      struct G9 { static constexpr const Sched& get() { return SS9; } };
      ring_gemm(IC<3>{}, IC<1>{}, IC<0>{}, SS9, G9{}, acc, cg, false, l16, [&](auto OP, auto KS, auto M) {
        constexpr int op = OP.value;
        if constexpr (op >= 2000 && op < 3000) ldw<op - 2000>(rsW, l16, rot5<op - 2000>(wdb, w >> 1));
        else if constexpr (op == 3000) set_flag<ops_after_code(SS9, 3023, KS.value, M.value)>(tag, rsW, flag_off(layer, PH_H, p, 0) + (g * 4 + w) * 8);
        else if constexpr (op == 3001) poll_issue(rsW, lane * 8, flag_off(layer, PH_H, p, 0));
        else if constexpr (op == 3002) poll_check<ops_after_code(SS9, 3001, KS.value, M.value)>(tag, rsW, lane * 8, flag_off(layer, PH_H, p, 0));
        else if constexpr (op >= 3010 && op < 3016) stats_stage(IC<op - 3010>{}, 1, tid);
        else if constexpr (op == 3020) {
#pragma unroll
          for (int i = 0; i < 3; ++i) sv[i] = *(const v4u*)(lds + LDS_H + (16 * mt + r) * HP + 16 * (w + 4 * i));
        } else if constexpr (op > 3020 && op <= 3023) {
          stc(sv[op - 3021], rsW, (unsigned)((mt >> 1) * 98304 + (mt & 1) * 1024 + r * 16), LB + L_H + p * 393216 + (3 * g + op - 3021) * 2048 + w * 256);
          asm volatile("" ::: "memory");
        } else if constexpr (op == 3030) {
#pragma unroll
          for (int m = 0; m < 4; ++m) st[m] = *(const f32x2*)(lds + LDS_ST + (64 + m * 16 + r) * 8);
        }
        else if constexpr (op >= 5000) { constexpr int i = op - 5000; af[i] = ldc_asm(rsW, (i & 1) ? lm1 : lm0, hb + (i >> 1) * 8192); }
      });
      STAMP(15);
      epilogue4(IC<1>{}, [&](auto I) {
        constexpr int i = I.value;
        if constexpr (i >= 1 && i <= 8) { af[6 + 2 * i] = ldc_asm(rsW, lm0, hb + (3 + i) * 8192); af[7 + 2 * i] = ldc_asm(rsW, lm1, hb + (3 + i) * 8192); }
        else if constexpr (i >= 9) sfor<3>([&](auto K) { constexpr int f = NBD9 + (i - 9) * 3 + K.value; ldw<f>(rsW, l16, rot5<f>(wdb, w >> 1)); });
      });
      sfor<60 - NBD9 - 12>([&](auto K) { constexpr int f = NBD9 + 12 + K.value; ldw<f>(rsW, l16, rot5<f>(wdb, w >> 1)); });
      STAMP(16);
    }
    {
      FRESH();
      unsigned hb = LB + L_H + p * 393216 + 196608 + rh * 98304 + w * 2048; OPAQUE(hb);
      unsigned wqb = WQ_OFF + (g * 160 + w) * 1024; OPAQUE(wqb);
      f32x4 acc[5][2];
      asm volatile("s_waitcnt vmcnt(14)" : "+v"(af[0]), "+v"(af[1]), "+v"(af[2]), "+v"(af[3]), "+v"(af[4]), "+v"(af[5]), "+v"(af[6]), "+v"(af[7]), "+v"(af[8]), "+v"(af[9]), "+v"(af[10]), "+v"(af[11]),
                   "+v"(af[12]), "+v"(af[13]), "+v"(af[14]), "+v"(af[15]), "+v"(af[16]), "+v"(af[17]), "+v"(af[18]), "+v"(af[19]), "+v"(af[20]), "+v"(af[21]), "+v"(af[22]), "+v"(af[23]) :: "memory");
      BAR();
      v4u sv[3];
      const int mt = (tid >> 4) & 3;
      sfor<12>([&](auto I) {
        constexpr int i = I.value;
        if constexpr (i == 8) set_flag<0>(tag, rsW, flag_off(layer, PH_H, p, 1) + (g * 4 + w) * 8);
        sfor<2>([&](auto M) { sfor<5>([&](auto N) {
          constexpr int m = M.value, n = N.value;
          if constexpr (i == 0) mfma_w0<n>(acc[n][m], af[m]); else mfma_w<i * 5 + n>(acc[n][m], af[i * 2 + m]);
          if constexpr (i == 0 && m == 0 && n < 3) sv[n] = *(const v4u*)(lds + LDS_H + 16384 + (16 * mt + r) * HP + 16 * (w + 4 * n));
          if constexpr (i == 1 && m == 0 && n < 3) {
            stc(sv[n], rsW, (unsigned)((mt >> 1) * 98304 + (mt & 1) * 1024 + r * 16), LB + L_H + p * 393216 + 196608 + (3 * g + n) * 2048 + w * 256);
            asm volatile("" ::: "memory");
          }
          SBAR();
        }); });
      });
      MFMA_DRAIN();
      STAMP(17);
      reduce_epilogue(IC<0>{}, acc, LB + L_X3 + p * 163840, LB + L_S3 + p * 16384, layer == 1, tid, w, p, g, [&](auto I) {
        if constexpr (I.value == 0) poll_issue(rsW, lane * 8, flag_off(layer, PH_H, p, 1));
        if constexpr (I.value == 2) {
          poll_check<0>(tag, rsW, lane * 8, flag_off(layer, PH_H, p, 1));
          STAMP(18);
#pragma unroll
          for (int i = 0; i < 5; ++i)
#pragma unroll
            for (int m = 0; m < 2; ++m) af[i * 2 + m] = ldc(rsW, l16 + (m ^ (w & 1)) * 1024, hb + i * 8192);
          asm volatile("" ::: "memory");
        }
      });
      STAMP(19);
      BAR();
      SBAR();
      sfor<12>([&](auto I) {
        constexpr int i = I.value;
        sfor<2>([&](auto M) {
          constexpr int m = M.value;
          sfor<5>([&](auto N) { constexpr int n = N.value; if constexpr (i == 0) mfma_w0<n>(acc[n][m], af[m]); else mfma_w<i * 5 + n>(acc[n][m], af[i * 2 + m]); });
          if constexpr (i < 7) { af[(i + 5) * 2 + m] = ldc(rsW, l16 + (m ^ (w & 1)) * 1024, hb + (i + 5) * 8192); asm volatile("" ::: "memory"); }
          if constexpr (i == 5 && m == 0) { if (layer == 0) set_flag<11>(tag, rsW, flag_off(layer, PH_X3, p, 0) + (g * 4 + w) * 8); }
          if constexpr (i >= 6) sfor<2>([&](auto K) { constexpr int k = (i - 6) * 4 + m * 2 + K.value; if constexpr (k < 20) ldw<k>(rsW, l16, wqb + k * 4096); });
          SBAR();
        });
      });
      MFMA_DRAIN();
      STAMP(20);
      unsigned ab = LB + L_X3 + p * 163840 + w * 1024, lb = LDS_R0 + w * 1024; OPAQUE(ab); OPAQUE(lb);
      reduce_epilogue(IC<1>{}, acc, LB + L_X3 + p * 163840, LB + L_S3 + p * 16384, layer == 1, tid, w, p, g, [&](auto I) {
        if (layer == 0) {
          if constexpr (I.value < 2) sfor<(NQ0 - 20) / 2>([&](auto K) { constexpr int k = 20 + I.value * ((NQ0 - 20) / 2) + K.value; ldw<k>(rsW, l16, wqb + k * 4096); });
          if constexpr (I.value == 1) poll_issue(rsW, lane * 8, flag_off(0, PH_X3, p, 0));
          if constexpr (I.value == 2) {
            poll_check<0>(tag, rsW, lane * 8, flag_off(0, PH_X3, p, 0));
            stats_issue(rsW, tid * 32, LB + L_S3 + p * 16384);
            sfor<NPRE>([&](auto K) { dma2<K.value * 4096>(rsW, l16, ab + K.value * 4096, lb); });
          }
        }
      });
      STAMP(21);
      STAMP(22);
    }
  }
}

extern "C" void kernel_launch(void* const* d_in, const int* in_sizes, int n_in, void* d_out, int out_size, void* d_ws,
                              size_t ws_size, hipStream_t stream) {
  const u16* x = (const u16*)d_in[0];
  (void)hipFuncSetAttribute((const void*)k_main, hipFuncAttributeMaxDynamicSharedMemorySize, LDS_BYTES);
  Wts W{x, (const u16*)d_in[1], (const u16*)d_in[2], (const u16*)d_in[4], (const u16*)d_in[5], (const u16*)d_in[6]};
  k_main<<<256, 256, LDS_BYTES, stream>>>(W, (u16*)d_out, (char*)d_ws);
}
```
